```python
import torch

def _softmax(x, axis=-1):
    return torch.softmax(x, dim=axis)
def _gelu(x):
    return torch.nn.functional.gelu(x, approximate="tanh")
def _silu(x):
    return torch.nn.functional.silu(x)
def _sigmoid(x):
    return torch.sigmoid(x)
def _layernorm(x, axis=-1, eps=1e-5):
    m = x.mean(dim=axis, keepdim=True)
    v = x.var(dim=axis, keepdim=True, unbiased=False)
    return (x - m) / torch.sqrt(v + eps)
def _scatter_add(tgt, idx, src, axis=0):
    return tgt.clone().index_add_(axis, idx, src)
def _segment_sum(src, seg, n):
    out = torch.zeros((n,) + src.shape[1:], dtype=src.dtype)
    return out.index_add_(0, seg, src)
def _pad(x, pw):
    flat = tuple(v for pair in reversed(pw) for v in pair)
    return torch.nn.functional.pad(x, flat)

kernel_name = 'interleave_layers__spectral_gfnet__linear_attn_block__prompt'

def setup_inputs():

    _w = (lambda a: (lambda t: t.to(torch.bfloat16) if t.is_floating_point() else t)(torch.as_tensor(a)))
    def _A_setup():
        B, S, D = 1, 2048, 576
        Df = D // 2 + 1
        return {
            "x": _w((lambda *s: torch.randn(*s, dtype=torch.bfloat16) * 0.05)(B, S, D)),
            "w_r": _w((lambda *s: torch.randn(*s, dtype=torch.bfloat16) * 0.4)(S, Df) * 0.02),
            "w_i": _w((lambda *s: torch.randn(*s, dtype=torch.bfloat16) * 0.4)(S, Df) * 0.02),
        }
    _A_in = _A_setup()

    def _B_setup():
        B, S, D = 1, 2048, 576
        return {
            "x": _w((lambda *s: torch.randn(*s, dtype=torch.bfloat16) * 0.05)(B, S, D)),
            "wqkv": _w((lambda *s: torch.randn(*s, dtype=torch.bfloat16) * 0.05)(D, 3 * D)),
            "wo": _w((lambda *s: torch.randn(*s, dtype=torch.bfloat16) * 0.05)(D, D)),
        }
    _B_in = _B_setup()

    return {
        "A_x": _A_in["x"],
        "A_w_r": _A_in["w_r"],
        "A_w_i": _A_in["w_i"],
        "B_x": _B_in["x"],
        "B_wqkv": _B_in["wqkv"],
        "B_wo": _B_in["wo"],
    }

def reference(A_x, A_w_r, A_w_i, B_x, B_wqkv, B_wo):
    def _A_ref(x, w_r, w_i):
        B, S, D = x.shape
        X = (lambda x, axes=(-2,-1): torch.fft.rfft2(x.float(), dim=axes))(x, axes=(-2, -1))
        W = w_r + 1j * w_i
        X = X * W
        y = (lambda x, s, axes=(-2,-1): torch.fft.irfft2(x, s=s, dim=axes).to(torch.bfloat16))(X, (S, D), axes=(-2, -1))
        return y

    def _B_ref(x, wqkv, wo):
        B, S, D = x.shape
        H = 32; Dh = D // H
        qkv = (lambda x, *d: x.permute(*d))((x @ wqkv).reshape(B, S, 3, H, Dh), 2, 0, 3, 1, 4)
        q, k, v = qkv[0], qkv[1], qkv[2]
        phi = lambda t: torch.where(t > 0, t + 1, torch.exp(t)) + 1e-6
        pq, pk = phi(q), phi(k)
        kv = torch.einsum("bhsd,bhse->bhde", pk, v)
        z = torch.einsum("bhsd->bhd", pk)
        num = torch.einsum("bhqd,bhde->bhqe", pq, kv)
        den = torch.einsum("bhqd,bhd->bhq", pq, z)[..., None] + 1e-6
        o = num / den
        return (lambda x, *d: x.permute(*d))(o, 0, 2, 1, 3).reshape(B, S, D) @ wo

    x = A_x
    x = x + _A_ref(_layernorm(x, axis=-1), A_w_r, A_w_i)
    x = x + _B_ref(_layernorm(x, axis=-1), B_wqkv, B_wo)
    x = x + _A_ref(_layernorm(x, axis=-1), A_w_r, A_w_i)
    x = x + _B_ref(_layernorm(x, axis=-1), B_wqkv, B_wo)
    return x
```

```cpp
#include <hip/hip_runtime.h>
#include <hip/hip_bf16.h>

typedef __bf16 bf16x8 __attribute__((ext_vector_type(8)));
typedef __bf16 bf16x2 __attribute__((ext_vector_type(2)));
typedef short s16x4 __attribute__((ext_vector_type(4)));
typedef float f32x4 __attribute__((ext_vector_type(4)));
typedef float f32x2 __attribute__((ext_vector_type(2)));
typedef unsigned u32x2 __attribute__((ext_vector_type(2)));
typedef unsigned u32x4 __attribute__((ext_vector_type(4)));
typedef unsigned short u16;
#define LDSP __attribute__((address_space(3)))
#define GLBP __attribute__((address_space(1)))
#define DI __device__ __forceinline__
#define FENCE() asm volatile("" ::: "memory")
#define WAITV(n) asm volatile("s_waitcnt vmcnt(" #n ")" ::: "memory")
#define BAR() do { asm volatile("s_waitcnt lgkmcnt(0)" ::: "memory"); __builtin_amdgcn_s_barrier(); FENCE(); } while (0)

constexpr int S = 2048, D = 576;
constexpr size_t XB = (size_t)S * D * 2;
constexpr size_t O_XH0 = 0, O_XH1 = XB, O_XH2 = 2 * XB, O_XH3 = 3 * XB, O_OB = 4 * XB, O_X3 = 5 * XB, O_WT = 6 * XB;
constexpr size_t O_QF = 7 * XB, O_WOQ = O_QF + 663552, O_WOF = O_WOQ + 663552, O_WF = O_WOF + 663552;
constexpr size_t O_QTF = O_WF + 2064384, O_SS0 = O_QTF + 884736, O_SS2 = O_SS0 + 8192, O_G = O_SS2 + 6 * 8192;
constexpr int GP = 2752;
constexpr size_t GL = (size_t)256 * GP;

DI f32x4 mfma(bf16x8 a, bf16x8 b, f32x4 c) { return __builtin_amdgcn_mfma_f32_16x16x32_bf16(a, b, c, 0, 0, 0); }
DI unsigned pk2(float a, float b) { f32x2 f = {a, b}; return __builtin_bit_cast(unsigned, __builtin_convertvector(f, bf16x2)); }
DI bf16x8 pk8(f32x4 a, f32x4 b) { u32x4 u = {pk2(a[0], a[1]), pk2(a[2], a[3]), pk2(b[0], b[1]), pk2(b[2], b[3])}; return __builtin_bit_cast(bf16x8, u); }
DI float lo16(unsigned u) { return __builtin_bit_cast(float, u << 16); }
DI float hi16(unsigned u) { return __builtin_bit_cast(float, u & 0xffff0000u); }
DI float cosr(float rev) { return __builtin_amdgcn_cosf(__builtin_amdgcn_fractf(rev)); }
DI float sinr(float rev) { return __builtin_amdgcn_sinf(__builtin_amdgcn_fractf(rev)); }
DI void glds(const char* g, char* l) { __builtin_amdgcn_global_load_lds((const GLBP void*)g, (LDSP void*)l, 16, 0, 0); }
DI bf16x8 ldf(const char* p) { return *(const bf16x8*)p; }
DI float qent(int d, int j) {
  int f = (j >> 1) + 1; float ph = (j & 1) ? 0.25f : 0.f, sc = 0.05892556509887896f;
  if (j >= 574) { sc = 0.041666666666666664f; ph = 0.f; f = (j == 575) ? 0 : 288; }
  return sc * cosr((float)(f * d) * (1.f / 576.f) + ph);
}
DI int operm(int k) {
  int hp = k / 36, c = k - 36 * hp, p = c >> 2, i = c & 3;
  return p < 8 ? 18 * (2 * hp + (p >> 2)) + 4 * (p & 3) + i : 18 * (2 * hp + (i >> 1)) + 16 + (i & 1);
}

template <int MODE>
DI void gemm3(char* sm, int rb, int ng, const char* xs, const char* wsrc, const char* res, char* out, float* ssq, char* qf_out) {
  const int tid = threadIdx.x, w = __builtin_amdgcn_readfirstlane(tid >> 6), l = tid & 63, g = l >> 4, m = l & 15;
  const int ws = MODE < 2 ? w : (w & 1), wn = MODE < 2 ? 0 : (w >> 1);
  const int s0 = (MODE < 2 ? 128 : 64) * rb + 32 * ws;
  const int t0 = (MODE < 2 ? 3 : 6) * ng;
  bf16x8 xf[2][18];
  u32x2 rv[2][3];
  if (MODE < 2) {
    int r0 = s0 + m, r1 = s0 + 16 + m;
    if (MODE == 1) { r0 = operm(r0 < 576 ? r0 : 0); r1 = operm(r1 < 576 ? r1 : 0); }
#pragma unroll
    for (int ks = 0; ks < 18; ++ks) {
      xf[0][ks] = ldf(xs + (size_t)r0 * 1152 + ks * 64 + g * 16);
      xf[1][ks] = ldf(xs + (size_t)r1 * 1152 + ks * 64 + g * 16);
    }
    FENCE();
    for (int q = w; q < 54; q += 4) {
      int t = q / 18, ks = q - 18 * t, j = 16 * (t0 + t) + m, d0 = 32 * ks + 8 * g;
      int f = (j >> 1) + 1; float ph = (j & 1) ? 0.25f : 0.f, sc = 0.05892556509887896f;
      if (j >= 574) { sc = 0.041666666666666664f; ph = 0.f; f = (j == 575) ? 0 : 288; }
      int i0 = (f * d0) % 576;
      float v[8];
#pragma unroll
      for (int i = 0; i < 8; ++i) v[i] = sc * __builtin_amdgcn_cosf((float)(i0 + f * i) * (1.f / 576.f) + ph);
      u32x4 u = {pk2(v[0], v[1]), pk2(v[2], v[3]), pk2(v[4], v[5]), pk2(v[6], v[7])};
      *(u32x4*)(sm + q * 1024 + l * 16) = u;
      if (MODE == 0 && rb == 0) *(u32x4*)(qf_out + (size_t)(t0 * 18 + q) * 1024 + l * 16) = u;
    }
    BAR();
  } else {
#pragma unroll
    for (int st = 0; st < 3; ++st) {
#pragma unroll
      for (int i = 0; i < 9; ++i) {
        int q = 4 * i + w, t = q % 6, ks = 6 * st + q / 6;
        glds(wsrc + (size_t)((t0 + t) * 18 + ks) * 1024 + l * 16, sm + (t * 18 + ks) * 1024);
      }
#pragma unroll
      for (int k = 0; k < 6; ++k) {
        int ks = 6 * st + k;
        xf[0][ks] = ldf(xs + ((size_t)(4 * ks + g) * S + s0 + m) * 16);
        xf[1][ks] = ldf(xs + ((size_t)(4 * ks + g) * S + s0 + 16 + m) * 16);
      }
      FENCE();
    }
#pragma unroll
    for (int a = 0; a < 2; ++a)
#pragma unroll
      for (int t = 0; t < 3; ++t) {
        int n = 16 * (t0 + 3 * wn + t) + 4 * g, s = s0 + 16 * a + m;
        rv[a][t] = MODE == 2 ? *(const u32x2*)(res + ((size_t)(n >> 3) * S + s) * 16 + (n & 4) * 2)
                             : *(const u32x2*)(res + (size_t)s * 1152 + n * 2);
      }
    FENCE();
  }
  f32x4 acc[2][3] = {}, gr[2] = {}, su[2] = {};
  const bool stats = MODE == 0 && ng == 0;
  const bf16x8 ones = __builtin_bit_cast(bf16x8, (u32x4){0x3f803f80u, 0x3f803f80u, 0x3f803f80u, 0x3f803f80u});
#pragma unroll
  for (int ks = 0; ks < 18; ++ks) {
    if (MODE >= 2) {
      if (ks == 0) { WAITV(48); BAR(); }
      if (ks == 6) { WAITV(27); BAR(); }
      if (ks == 12) { WAITV(6); BAR(); }
    }
#pragma unroll
    for (int t = 0; t < 3; ++t) {
      bf16x8 wf = ldf(sm + ((3 * wn + t) * 18 + ks) * 1024 + l * 16);
#pragma unroll
      for (int a = 0; a < 2; ++a) acc[a][t] = MODE == 1 ? mfma(xf[a][ks], wf, acc[a][t]) : mfma(wf, xf[a][ks], acc[a][t]);
    }
    if (stats) {
#pragma unroll
      for (int a = 0; a < 2; ++a) { gr[a] = mfma(xf[a][ks], xf[a][ks], gr[a]); su[a] = mfma(ones, xf[a][ks], su[a]); }
    }
  }
  if (MODE == 1) {
    if (s0 < 576) {
#pragma unroll
      for (int a = 0; a < 2; ++a)
#pragma unroll
        for (int t = 0; t < 3; ++t) {
          int kt = (s0 >> 4) + a;
          u32x2 u = {pk2(acc[a][t][0], acc[a][t][1]), pk2(acc[a][t][2], acc[a][t][3])};
          *(u32x2*)(out + (size_t)((t0 + t) * 18 + (kt >> 1)) * 1024 + (m + 16 * (2 * (kt & 1) + (g >> 1))) * 16 + (g & 1) * 8) = u;
        }
    }
    return;
  }
  float sq[2] = {0.f, 0.f};
#pragma unroll
  for (int a = 0; a < 2; ++a)
#pragma unroll
    for (int t = 0; t < 3; ++t) {
      int n = 16 * (t0 + 3 * wn + t) + 4 * g, s = s0 + 16 * a + m;
      f32x4 v = acc[a][t];
      if (MODE >= 2) { v[0] += lo16(rv[a][t][0]); v[1] += hi16(rv[a][t][0]); v[2] += lo16(rv[a][t][1]); v[3] += hi16(rv[a][t][1]); }
      u32x2 u = {pk2(v[0], v[1]), pk2(v[2], v[3])};
      if (MODE == 3) *(u32x2*)(out + (size_t)s * 1152 + n * 2) = u;
      else *(u32x2*)(out + ((size_t)(n >> 3) * S + s) * 16 + (n & 4) * 2) = u;
      if (MODE == 2) sq[a] += v[0] * v[0] + v[1] * v[1] + v[2] * v[2] + (n == 572 ? 0.f : v[3] * v[3]);
    }
  if (stats) {
#pragma unroll
    for (int a = 0; a < 2; ++a) {
      float dsel = (m & 3) == 0 ? gr[a][0] : (m & 3) == 1 ? gr[a][1] : (m & 3) == 2 ? gr[a][2] : gr[a][3];
      if ((m >> 2) == g) ssq[s0 + 16 * a + m] = fmaxf(dsel - su[a][0] * su[a][0] * (1.f / 576.f), 0.f);
    }
  }
  if (MODE == 2) {
#pragma unroll
    for (int a = 0; a < 2; ++a) { sq[a] += __shfl_xor(sq[a], 16); sq[a] += __shfl_xor(sq[a], 32); }
    float* red = (float*)sm;
    BAR();
    if (wn == 1 && g < 2) red[ws * 32 + 16 * g + m] = g ? sq[1] : sq[0];
    BAR();
    if (wn == 0 && g < 2) ssq[ng * S + s0 + 16 * g + m] = (g ? sq[1] : sq[0]) + red[ws * 32 + 16 * g + m];
  }
}

__global__ void __launch_bounds__(256, 1) k_pre(const char* x0, const char* wo, const char* wr, const char* wi, char* ws) {
  extern __shared__ __attribute__((aligned(16))) char sm[];
  const int b = blockIdx.x, tid = threadIdx.x;
  if (b < 192) {
    int x = b & 7, i = b >> 3;
    gemm3<0>(sm, 2 * x + (i & 1), i >> 1, x0, nullptr, nullptr, ws + O_XH0, (float*)(ws + O_SS0), ws + O_QF);
  } else if (b < 252) {
    int u = b - 192;
    gemm3<1>(sm, u % 5, u / 5, wo, nullptr, nullptr, ws + O_WOQ, nullptr, nullptr);
  } else {
    int u = b - 252, tg = u >> 3, kb = 8 * (u & 7);
    u32x4 v[10];
#pragma unroll
    for (int i = 0; i < 10; ++i) {
      int e = i * 256 + tid; e = e < 2312 ? e : 2311;
      int run = e / 289, o = e - 289 * run;
      v[i] = *(const u32x4*)((run < 4 ? wr : wi) + (size_t)(kb + 64 * (4 * tg + (run & 3))) * 578 + o * 16);
    }
#pragma unroll
    for (int i = 0; i < 10; ++i) { int e = i * 256 + tid; e = e < 2312 ? e : 2311; *(u32x4*)(sm + e * 16) = v[i]; }
    __syncthreads();
#pragma unroll
    for (int i = 0; i < 9; ++i) {
      int e = i * 256 + tid, mm = e & 7, c = e >> 3;
      const char* p = sm + mm * 578 + 2 * (c + 1);
      u32x4 o;
#pragma unroll
      for (int r = 0; r < 4; ++r) o[r] = (unsigned)*(const u16*)(p + r * 4624) | ((unsigned)*(const u16*)(p + (4 + r) * 4624) << 16);
      *(u32x4*)(ws + O_WT + (size_t)((c * 4 + (kb >> 4)) * 2 + (tg >> 2)) * 1024 + (16 * (tg & 3) + (kb & 8) + mm) * 16) = o;
    }
  }
}

template <int NP>
DI void fft_cols(char* sm, int b, const char* xin, char* xout, const float* ssq, const char* wt) {
  const int tid = threadIdx.x, w = __builtin_amdgcn_readfirstlane(tid >> 6), l = tid & 63, g = l >> 4, m = l & 15;
  const int kb = (b & 7) + 8 * (b >> 4), half = (b >> 3) & 1;
  const bool last = kb == 71 && half == 1;
  f32x4 ps[NP][2];
#pragma unroll
  for (int p = 0; p < NP; ++p) { ps[p][0] = *(const f32x4*)(ssq + p * S + tid * 8); ps[p][1] = *(const f32x4*)(ssq + p * S + tid * 8 + 4); }
  u32x2 xv[2][4];
  const size_t xo = ((size_t)kb * S + 32 * (16 * w + 4 * g) + m) * 16 + 8 * half;
#pragma unroll
  for (int t = 0; t < 2; ++t)
#pragma unroll
    for (int r = 0; r < 4; ++r) xv[t][r] = *(const u32x2*)(xin + xo + (32 * r + 16 * t) * 16);
  u32x4 wv[2][2];
#pragma unroll
  for (int c = 0; c < 2; ++c)
#pragma unroll
    for (int t = 0; t < 2; ++t) wv[c][t] = *(const u32x4*)(wt + (size_t)(((4 * kb + 2 * half + c) * 4 + w) * 2 + t) * 1024 + l * 16);
  FENCE();
  bf16x8 c64[2], s64[2], n64[2], c32[2], s32[2], n32[2];
  float t1c[2][4], t1s[2][4], t2c[2][4], t2s[2][4];
#pragma unroll
  for (int h = 0; h < 2; ++h) {
    f32x4 c[2], s[2], cc[2], ss[2];
#pragma unroll
    for (int j = 0; j < 8; ++j) {
      float p = (float)(((32 * h + 8 * g + j) * (16 * w + m)) & 63) * (1.f / 64.f);
      c[j >> 2][j & 3] = __builtin_amdgcn_cosf(p); s[j >> 2][j & 3] = __builtin_amdgcn_sinf(p);
      float q = (float)(((16 * h + m) * (16 * (j >> 2) + 4 * g + (j & 3))) & 31) * (1.f / 32.f);
      cc[j >> 2][j & 3] = __builtin_amdgcn_cosf(q); ss[j >> 2][j & 3] = __builtin_amdgcn_sinf(q);
    }
    c64[h] = pk8(c[0], c[1]); s64[h] = pk8(s[0], s[1]); n64[h] = pk8(-s[0], -s[1]);
    c32[h] = pk8(cc[0], cc[1]); s32[h] = pk8(ss[0], ss[1]); n32[h] = pk8(-ss[0], -ss[1]);
#pragma unroll
    for (int r = 0; r < 4; ++r) {
      float p = (float)((16 * h + 4 * g + r) * (16 * w + m)) * (1.f / 2048.f);
      t1c[h][r] = __builtin_amdgcn_cosf(p); t1s[h][r] = __builtin_amdgcn_sinf(p);
      float q = (float)((16 * h + m) * (16 * w + 4 * g + r)) * (1.f / 2048.f);
      t2c[h][r] = __builtin_amdgcn_cosf(q) * (1.f / 2048.f); t2s[h][r] = __builtin_amdgcn_sinf(q) * (1.f / 2048.f);
    }
  }
  float* rsl = (float*)(sm + 32768);
  {
    f32x4 a = ps[0][0], c = ps[0][1];
#pragma unroll
    for (int p = 1; p < NP; ++p) { a += ps[p][0]; c += ps[p][1]; }
#pragma unroll
    for (int i = 0; i < 4; ++i) { a[i] = __builtin_amdgcn_rsqf(a[i] * (1.f / 576.f) + 1e-5f); c[i] = __builtin_amdgcn_rsqf(c[i] * (1.f / 576.f) + 1e-5f); }
    *(f32x4*)(rsl + tid * 8) = a; *(f32x4*)(rsl + tid * 8 + 4) = c;
  }
  BAR();
  const int wo = m * 128 + (((2 * w + (g >> 1)) ^ (m & 7)) << 4) + (g & 1) * 8;
#pragma unroll
  for (int t = 0; t < 2; ++t) {
    float u[4][4];
#pragma unroll
    for (int r = 0; r < 4; ++r) {
      float rs = rsl[32 * (16 * w + 4 * g + r) + 16 * t + m];
      u[0][r] = lo16(xv[t][r][0]) * rs; u[1][r] = hi16(xv[t][r][0]) * rs; u[2][r] = lo16(xv[t][r][1]) * rs;
      u[3][r] = last ? 0.f : hi16(xv[t][r][1]) * rs;
    }
#pragma unroll
    for (int p = 0; p < 4; ++p) *(u32x2*)(sm + p * 4096 + t * 2048 + wo) = (u32x2){pk2(u[p][0], u[p][1]), pk2(u[p][2], u[p][3])};
  }
  BAR();
  const int ro0 = m * 128 + ((g ^ (m & 7)) << 4), ro1 = m * 128 + (((4 + g) ^ (m & 7)) << 4);
  const f32x4 z4 = {0.f, 0.f, 0.f, 0.f};
#pragma unroll
  for (int c = 0; c < 2; ++c) {
    f32x4 re[2], im[2];
#pragma unroll
    for (int t = 0; t < 2; ++t) {
      const char* p = sm + c * 8192 + t * 2048;
      bf16x8 a0 = ldf(p + ro0), a1 = ldf(p + ro1), b0 = ldf(p + 4096 + ro0), b1 = ldf(p + 4096 + ro1);
      f32x4 x = mfma(a0, c64[0], z4), y = mfma(b0, c64[0], z4);
      x = mfma(a1, c64[1], x); y = mfma(b1, c64[1], y);
      x = mfma(b0, s64[0], x); y = mfma(a0, n64[0], y);
      x = mfma(b1, s64[1], x); y = mfma(a1, n64[1], y);
#pragma unroll
      for (int r = 0; r < 4; ++r) { re[t][r] = x[r] * t1c[t][r] + y[r] * t1s[t][r]; im[t][r] = y[r] * t1c[t][r] - x[r] * t1s[t][r]; }
    }
    bf16x8 fr = pk8(re[0], re[1]), fi = pk8(im[0], im[1]);
#pragma unroll
    for (int t = 0; t < 2; ++t) {
      f32x4 x = mfma(c32[t], fr, z4), y = mfma(c32[t], fi, z4);
      x = mfma(s32[t], fi, x); y = mfma(n32[t], fr, y);
#pragma unroll
      for (int r = 0; r < 4; ++r) {
        float a = lo16(wv[c][t][r]), bb = hi16(wv[c][t][r]);
        re[t][r] = x[r] * a - y[r] * bb; im[t][r] = x[r] * bb + y[r] * a;
      }
    }
    fr = pk8(re[0], re[1]); fi = pk8(im[0], im[1]);
#pragma unroll
    for (int t = 0; t < 2; ++t) {
      f32x4 x = mfma(fr, c32[t], z4), y = mfma(fi, c32[t], z4);
      x = mfma(fi, n32[t], x); y = mfma(fr, s32[t], y);
      float vr[4], vi[4];
#pragma unroll
      for (int r = 0; r < 4; ++r) { vr[r] = x[r] * t2c[t][r] - y[r] * t2s[t][r]; vi[r] = y[r] * t2c[t][r] + x[r] * t2s[t][r]; }
      char* p = sm + 16384 + c * 8192 + t * 2048 + wo;
      *(u32x2*)p = (u32x2){pk2(vr[0], vr[1]), pk2(vr[2], vr[3])};
      *(u32x2*)(p + 4096) = (u32x2){pk2(vi[0], vi[1]), pk2(vi[2], vi[3])};
    }
  }
  BAR();
#pragma unroll
  for (int t = 0; t < 2; ++t) {
    f32x4 yr[2], yi[2];
#pragma unroll
    for (int c = 0; c < 2; ++c) {
      const char* p = sm + 16384 + c * 8192 + t * 2048;
      bf16x8 a0 = ldf(p + ro0), a1 = ldf(p + ro1), b0 = ldf(p + 4096 + ro0), b1 = ldf(p + 4096 + ro1);
      f32x4 x = mfma(c64[0], a0, z4), y = mfma(c64[0], b0, z4);
      x = mfma(c64[1], a1, x); y = mfma(c64[1], b1, y);
      x = mfma(n64[0], b0, x); y = mfma(s64[0], a0, y);
      x = mfma(n64[1], b1, x); y = mfma(s64[1], a1, y);
      yr[c] = x; yi[c] = y;
    }
#pragma unroll
    for (int r = 0; r < 4; ++r) {
      u32x2 o = {pk2(lo16(xv[t][r][0]) + yr[0][r], hi16(xv[t][r][0]) + yi[0][r]),
                 pk2(lo16(xv[t][r][1]) + yr[1][r], hi16(xv[t][r][1]) + (last ? 0.f : yi[1][r]))};
      *(u32x2*)(xout + xo + (32 * r + 16 * t) * 16) = o;
    }
  }
}

DI void wqkv_rot(char* sm, int u, const char* wqkv, const char* qf, char* wf) {
  const int tid = threadIdx.x, w = __builtin_amdgcn_readfirstlane(tid >> 6), l = tid & 63, g = l >> 4, m = l & 15;
  const int jg = u % 5, ng = u / 5, jt0 = 8 * jg + 2 * w;
  const bool act = jt0 < 36;
  {
    int tt = (tid >> 2) & 3, p = tid & 3, row0 = tid >> 4, T = 4 * ng + tt, hp = T / 7, cc = 16 * (T - 7 * hp) + 4 * p;
    int col = cc < 36 ? 576 + cc : cc < 72 ? 1152 + cc - 36 : cc - 72;
    bool ok = cc < 108;
    const char* src = wqkv + (size_t)row0 * 3456 + (ok ? col + 36 * hp : 0) * 2;
    char* dst = sm + row0 * 128 + ((tt ^ (((row0 >> 1) & 1) | ((row0 >> 2) & 2))) << 5) + 8 * p;
    u32x2 v[36];
#pragma unroll
    for (int i = 0; i < 36; ++i) v[i] = *(const u32x2*)(src + (size_t)i * 16 * 3456);
    FENCE();
#pragma unroll
    for (int i = 0; i < 36; ++i) *(u32x2*)(dst + i * 2048) = ok ? v[i] : (u32x2){0u, 0u};
  }
  bf16x8 af[2][18];
  const int jl = act ? jt0 : 0;
#pragma unroll
  for (int ks = 0; ks < 18; ++ks) { af[0][ks] = ldf(qf + (size_t)(jl * 18 + ks) * 1024 + l * 16); af[1][ks] = ldf(qf + (size_t)((jl + 1) * 18 + ks) * 1024 + l * 16); }
  BAR();
  f32x4 acc[2][4] = {};
  const int rbase = (8 * g + (m >> 2)) * 128 + 8 * (m & 3), fx = ((m >> 3) & 1) | ((g & 1) << 1);
#pragma unroll
  for (int ks = 0; ks < 18; ++ks)
#pragma unroll
    for (int t = 0; t < 4; ++t) {
      char* p = sm + ks * 4096 + rbase + ((t ^ fx) << 5);
      s16x4 lo = __builtin_amdgcn_ds_read_tr16_b64_v4i16((LDSP s16x4*)p), hi = __builtin_amdgcn_ds_read_tr16_b64_v4i16((LDSP s16x4*)(p + 512));
      bf16x8 bfr = __builtin_bit_cast(bf16x8, __builtin_shufflevector(lo, hi, 0, 1, 2, 3, 4, 5, 6, 7));
      acc[0][t] = mfma(af[0][ks], bfr, acc[0][t]); acc[1][t] = mfma(af[1][ks], bfr, acc[1][t]);
    }
  if (act) {
#pragma unroll
    for (int a = 0; a < 2; ++a)
#pragma unroll
      for (int t = 0; t < 4; ++t) {
        int jt = jt0 + a;
        float v3 = (jt == 35 && g == 3) ? 0.f : acc[a][t][3];
        u32x2 o = {pk2(acc[a][t][0], acc[a][t][1]), pk2(acc[a][t][2], v3)};
        *(u32x2*)(wf + (size_t)((4 * ng + t) * 18 + (jt >> 1)) * 1024 + (m + 16 * (2 * (jt & 1) + (g >> 1))) * 16 + (g & 1) * 8) = o;
      }
  }
}

__global__ void __launch_bounds__(256, 1) k_fft1(const char* wqkv, const char* wo, char* ws) {
  extern __shared__ __attribute__((aligned(16))) char sm[];
  const int b = blockIdx.x, tid = threadIdx.x;
  if (b < 144) fft_cols<1>(sm, b, ws + O_XH0, ws + O_XH1, (const float*)(ws + O_SS0), ws + O_WT);
  else if (b < 284) wqkv_rot(sm, b - 144, wqkv, ws + O_QF, ws + O_WF);
  else {
    const int id = (b - 284) * 256 + tid;
#pragma unroll
    for (int i = 0; i < 3; ++i) {
      int e = i * 13824 + id, l = e & 63, pc = e >> 6, nt = pc / 18, ks = pc - 18 * nt;
      unsigned h[8];
#pragma unroll
      for (int j = 0; j < 8; ++j) h[j] = *(const u16*)(wo + (size_t)operm(32 * ks + 8 * (l >> 4) + j) * 1152 + (16 * nt + (l & 15)) * 2);
      *(u32x4*)(ws + O_WOF + (size_t)e * 16) = (u32x4){h[0] | (h[1] << 16), h[2] | (h[3] << 16), h[4] | (h[5] << 16), h[6] | (h[7] << 16)};
    }
#pragma unroll
    for (int i = 0; i < 4; ++i) {
      int e = i * 13824 + id, l = e & 63, pc = e >> 6, nt = pc / 18, ks = pc - 18 * nt, hp = nt / 3, c = 16 * (nt - 3 * hp) + (l & 15);
      int d = 36 * hp + (c < 36 ? c : 0), j0 = 32 * ks + 8 * (l >> 4);
      *(u32x4*)(ws + O_QTF + (size_t)e * 16) = (u32x4){pk2(qent(d, j0), qent(d, j0 + 1)), pk2(qent(d, j0 + 2), qent(d, j0 + 3)),
                                                       pk2(qent(d, j0 + 4), qent(d, j0 + 5)), pk2(qent(d, j0 + 6), qent(d, j0 + 7))};
    }
    for (int e = id; e < (int)(2 * GL / 16); e += 13824) *(u32x4*)(ws + O_G + (size_t)e * 16) = (u32x4){0u, 0u, 0u, 0u};
  }
}
__global__ void __launch_bounds__(256, 1) k_fft2(char* ws) {
  extern __shared__ __attribute__((aligned(16))) char sm[];
  fft_cols<6>(sm, blockIdx.x, ws + O_XH2, ws + O_XH3, (const float*)(ws + O_SS2), ws + O_WT);
}

DI float phi(float t) { return (t > 0.f ? t + 1.f : __expf(t)) + 1e-6f; }
template <int LAST>
__global__ void __launch_bounds__(256, 1) k_attn(const char* xin, char* ws) {
  extern __shared__ __attribute__((aligned(16))) char sm[];
  const int tid = threadIdx.x, w = __builtin_amdgcn_readfirstlane(tid >> 6), l = tid & 63, g = l >> 4, m = l & 15;
  const int b = blockIdx.x, rb = 4 * ((b & 7) >> 1) + ((b >> 3) & 3), hp = 8 * (b & 1) + (b >> 5);
  const int s0 = 128 * rb + 32 * w;
  const char* wsrc = ws + O_WF + (size_t)hp * 7 * 18 * 1024 + l * 16;
  bf16x8 xf[2][18];
#pragma unroll
  for (int st = 0; st < 5; ++st) {
#pragma unroll
    for (int i = 0; i < (st < 4 ? 7 : 4); ++i) {
      int q = 4 * i + w; if (st == 4 && q >= 14) q -= 2;
      int t = q % 7, ks = 4 * st + q / 7;
      glds(wsrc + (t * 18 + ks) * 1024, sm + (t * 18 + ks) * 1024);
    }
#pragma unroll
    for (int k = 0; k < (st < 4 ? 4 : 2); ++k) {
      int ks = 4 * st + k;
      xf[0][ks] = ldf(xin + ((size_t)(4 * ks + g) * S + s0 + m) * 16);
      xf[1][ks] = ldf(xin + ((size_t)(4 * ks + g) * S + s0 + 16 + m) * 16);
    }
    FENCE();
  }
  f32x4 acc[2][7] = {}, gr[2] = {};
#pragma unroll
  for (int ks = 0; ks < 18; ++ks) {
    if (ks == 0) { WAITV(53); BAR(); }
    if (ks == 4) { WAITV(38); BAR(); }
    if (ks == 8) { WAITV(23); BAR(); }
    if (ks == 12) { WAITV(8); BAR(); }
    if (ks == 16) { WAITV(0); BAR(); }
#pragma unroll
    for (int t = 0; t < 7; ++t) {
      bf16x8 wf = ldf(sm + (t * 18 + ks) * 1024 + l * 16);
      acc[0][t] = mfma(xf[0][ks], wf, acc[0][t]); acc[1][t] = mfma(xf[1][ks], wf, acc[1][t]);
    }
#pragma unroll
    for (int a = 0; a < 2; ++a) {
      bf16x8 xm = xf[a][ks];
      if (ks == 17) { u32x4 u = __builtin_bit_cast(u32x4, xm); u[3] = g == 3 ? (u[3] & 0xffffu) : u[3]; xm = __builtin_bit_cast(bf16x8, u); }
      gr[a] = mfma(xm, xm, gr[a]);
    }
  }
#pragma unroll
  for (int a = 0; a < 2; ++a)
#pragma unroll
    for (int r = 0; r < 4; ++r) {
      float rs = __builtin_amdgcn_rsqf(__shfl(gr[a][r], 20 * g + r) * (1.f / 576.f) + 1e-5f);
#pragma unroll
      for (int t = 0; t < 7; ++t) acc[a][t][r] *= rs;
    }
  bf16x8 pkf[3], vf[3];
#pragma unroll
  for (int t = 0; t < 3; ++t) {
    f32x4 p0, p1;
#pragma unroll
    for (int r = 0; r < 4; ++r) { p0[r] = phi(acc[0][t][r]); p1[r] = phi(acc[1][t][r]); }
    pkf[t] = pk8(p0, p1); vf[t] = pk8(acc[0][2 + t], acc[1][2 + t]);
  }
  if (m == 8) vf[2] = __builtin_bit_cast(bf16x8, (u32x4){0x3f803f80u, 0x3f803f80u, 0x3f803f80u, 0x3f803f80u});
  const f32x4 z4 = {0.f, 0.f, 0.f, 0.f};
  BAR();
  {
    char* red = sm + w * 8192 + l * 16;
#pragma unroll
    for (int p = 0; p < 8; ++p) { int a = p < 6 ? p / 3 : 2, bt = p < 6 ? p % 3 : p - 5; *(f32x4*)(red + p * 1024) = mfma(pkf[a], vf[bt], z4); }
    *(u32x4*)(sm + 32768 + tid * 16) = (u32x4){0u, 0u, 0u, 0u};
  }
  BAR();
  char* gbase = ws + O_G + (LAST ? GL : 0) + (size_t)hp * 16 * GP;
#pragma unroll
  for (int i = 0; i < 2; ++i) {
    int ga = tid + 256 * i;
    if (ga < 342) {
      int he = ga / 9, dp = ga - 9 * he, h = he >= 19, e = he - 19 * h;
      int dk = 18 * h + 2 * dp, col = e < 18 ? 36 + 18 * h + e : 72, a = dk >> 4;
      const char* p = sm + (3 * a + (col >> 4) - 2 - (a == 2)) * 1024 + ((col & 15) + 16 * ((dk & 15) >> 2)) * 16 + (dk & 3) * 4;
      f32x2 v = *(const f32x2*)p + *(const f32x2*)(p + 8192) + (*(const f32x2*)(p + 16384) + *(const f32x2*)(p + 24576));
      unsigned u0 = ((__float_as_uint(v[0]) + 0x80u) & 0xffffff00u) | 0x5au, u1 = ((__float_as_uint(v[1]) + 0x80u) & 0xffffff00u) | 0x5au;
      __hip_atomic_store((GLBP unsigned long long*)(gbase + rb * GP + ga * 8), ((unsigned long long)u1 << 32) | u0, __ATOMIC_RELAXED, __HIP_MEMORY_SCOPE_AGENT);
    }
  }
  char* pq = sm + 36864 + w * 4096;
#pragma unroll
  for (int i = 0; i < 4; ++i) *(u32x4*)(pq + i * 1024 + l * 16) = (u32x4){0u, 0u, 0u, 0u};
  FENCE();
#pragma unroll
  for (int t = 4; t < 7; ++t) {
    int qc = 16 * t + m - 72, off = (qc >= 18 ? qc + 14 : qc) * 2;
    if (qc >= 0 && qc < 36) {
#pragma unroll
      for (int a = 0; a < 2; ++a)
#pragma unroll
        for (int r = 0; r < 4; ++r) {
          int row = 16 * a + 4 * g + r;
          *(u16*)(pq + row * 128 + (off ^ ((row & 7) << 4))) = (u16)pk2(phi(acc[a][t][r]), 0.f);
        }
    }
  }
  if (LAST) {
    const char* qsrc = ws + O_QTF + (size_t)hp * 54 * 1024 + l * 16;
    f32x4 y[2][3] = {};
#pragma unroll
    for (int t = 0; t < 3; ++t) {
      bf16x8 qf[18];
#pragma unroll
      for (int ks = 0; ks < 18; ++ks) qf[ks] = ldf(qsrc + (t * 18 + ks) * 1024);
#pragma unroll
      for (int ks = 0; ks < 18; ++ks) { y[0][t] = mfma(qf[ks], xf[0][ks], y[0][t]); y[1][t] = mfma(qf[ks], xf[1][ks], y[1][t]); }
      if (t < 2 || g == 0) {
#pragma unroll
        for (int a = 0; a < 2; ++a)
          *(u32x2*)(ws + O_X3 + (size_t)(s0 + 16 * a + m) * 1152 + (36 * hp + 16 * t + 4 * g) * 2) = (u32x2){pk2(y[a][t][0], y[a][t][1]), pk2(y[a][t][2], y[a][t][3])};
      }
    }
  }
  if (w < 3) {
    __amdgpu_buffer_rsrc_t rs = __builtin_amdgcn_make_buffer_rsrc(gbase, 0, 16 * GP, 0x00020000);
    const int o = (tid < 171 ? tid : 170) * 16;
    u32x4 v[16];
    for (int spin = 0; spin < 200000; ++spin) {
      FENCE();
      unsigned ok = 0xffu;
#pragma unroll
      for (int p = 0; p < 16; ++p) v[p] = __builtin_amdgcn_raw_buffer_load_b128(rs, o + p * GP, 0, 16);
#pragma unroll
      for (int p = 0; p < 16; ++p) ok &= (v[p][0] & v[p][1]) & (v[p][2] & v[p][3]);
      if (__all((ok & 0xffu) == 0x5au)) break;
      __builtin_amdgcn_s_sleep(2);
    }
    f32x4 sum = z4;
#pragma unroll
    for (int p = 0; p < 16; ++p) sum += __builtin_bit_cast(f32x4, v[p] & 0xffffff00u);
    if (tid < 171) {
#pragma unroll
      for (int i = 0; i < 2; ++i) {
        int ga = 2 * tid + i, he = ga / 9, dp = ga - 9 * he, row = he >= 19 ? he + 13 : he;
        *(unsigned*)(sm + 32768 + row * 64 + dp * 4) = pk2(sum[2 * i], sum[2 * i + 1]);
      }
    }
  }
  BAR();
#pragma unroll
  for (int a = 0; a < 2; ++a) {
    unsigned tail[2];
    char* ob = ws + O_OB + (size_t)(s0 + 16 * a + m) * 16;
#pragma unroll
    for (int h = 0; h < 2; ++h) {
      bf16x8 k0 = ldf(sm + 32768 + (32 * h + m) * 64 + g * 16), k1 = ldf(sm + 32768 + (32 * h + 16 + m) * 64 + g * 16);
      bf16x8 pf = ldf(pq + (16 * a + m) * 128 + ((h * 64 + g * 16) ^ ((m & 7) << 4)));
      f32x4 n0 = mfma(k0, pf, z4), n1 = mfma(k1, pf, z4);
      float inv = 1.f / (__shfl(n1[2], m) + 1e-6f);
      int P = 9 * hp + 4 * h + g;
      *(u32x2*)(ob + (size_t)(P >> 1) * S * 16 + (P & 1) * 8) = (u32x2){pk2(n0[0] * inv, n0[1] * inv), pk2(n0[2] * inv, n0[3] * inv)};
      tail[h] = pk2(n1[0] * inv, n1[1] * inv);
    }
    int P = 9 * hp + 8;
    if (g == 0) *(u32x2*)(ob + (size_t)(P >> 1) * S * 16 + (P & 1) * 8) = (u32x2){tail[0], tail[1]};
  }
}

template <int MODE>
__global__ void __launch_bounds__(256, 1) k_out(char* ws, char* out) {
  extern __shared__ __attribute__((aligned(16))) char sm[];
  const int b = blockIdx.x, x = b & 7, i = b >> 3;
  const int rb = 8 * (x >> 1) + (i & 7), ng = 3 * (x & 1) + (i >> 3);
  if (MODE == 2) gemm3<2>(sm, rb, ng, ws + O_OB, ws + O_WOQ, ws + O_XH1, ws + O_XH2, (float*)(ws + O_SS2), nullptr);
  else gemm3<3>(sm, rb, ng, ws + O_OB, ws + O_WOF, ws + O_X3, out, nullptr, nullptr);
}

extern "C" void kernel_launch(void* const* d_in, const int* in_sizes, int n_in, void* d_out, int out_size, void* d_ws,
                              size_t ws_size, hipStream_t stream) {
  const char *x0 = (const char*)d_in[0], *wr = (const char*)d_in[1], *wi = (const char*)d_in[2];
  const char *wqkv = (const char*)d_in[4], *wo = (const char*)d_in[5];
  char* ws = (char*)d_ws;
  (void)hipFuncSetAttribute((const void*)k_fft1, hipFuncAttributeMaxDynamicSharedMemorySize, 73728);
  (void)hipFuncSetAttribute((const void*)k_attn<0>, hipFuncAttributeMaxDynamicSharedMemorySize, 129024);
  (void)hipFuncSetAttribute((const void*)k_attn<1>, hipFuncAttributeMaxDynamicSharedMemorySize, 129024);
  (void)hipFuncSetAttribute((const void*)k_out<2>, hipFuncAttributeMaxDynamicSharedMemorySize, 110592);
  (void)hipFuncSetAttribute((const void*)k_out<3>, hipFuncAttributeMaxDynamicSharedMemorySize, 110592);
  k_pre<<<316, 256, 55296, stream>>>(x0, wo, wr, wi, ws);
  k_fft1<<<338, 256, 73728, stream>>>(wqkv, wo, ws);
  k_attn<0><<<256, 256, 129024, stream>>>(ws + O_XH1, ws);
  k_out<2><<<192, 256, 110592, stream>>>(ws, (char*)d_out);
  k_fft2<<<144, 256, 40960, stream>>>(ws);
  k_attn<1><<<256, 256, 129024, stream>>>(ws + O_XH3, ws);
  k_out<3><<<192, 256, 110592, stream>>>(ws, (char*)d_out);
}
```

```cpp
#include <hip/hip_runtime.h>
#include <hip/hip_bf16.h>

typedef __bf16 bf16x8 __attribute__((ext_vector_type(8)));
typedef __bf16 bf16x2 __attribute__((ext_vector_type(2)));
typedef short s16x4 __attribute__((ext_vector_type(4)));
typedef float f32x4 __attribute__((ext_vector_type(4)));
typedef float f32x2 __attribute__((ext_vector_type(2)));
typedef unsigned u32x2 __attribute__((ext_vector_type(2)));
typedef unsigned u32x4 __attribute__((ext_vector_type(4)));
typedef unsigned short u16;
#define LDSP __attribute__((address_space(3)))
#define GLBP __attribute__((address_space(1)))
#define DI __device__ __forceinline__
#define FENCE() asm volatile("" ::: "memory")
#define WAITV(n) asm volatile("s_waitcnt vmcnt(" #n ")" ::: "memory")
#define SB() __builtin_amdgcn_sched_barrier(0)
#define BAR() do { SB(); asm volatile("s_waitcnt lgkmcnt(0)" ::: "memory"); __builtin_amdgcn_s_barrier(); FENCE(); SB(); } while (0)

#define STAMP(k, i) do {} while (0)
constexpr int S = 2048, D = 576;
constexpr size_t XB = (size_t)S * D * 2;
constexpr size_t O_XH0 = 0, O_XH1 = XB, O_XH2 = 2 * XB, O_XH3 = 3 * XB, O_OB = 4 * XB, O_X3 = 5 * XB, O_WT = 6 * XB;
constexpr size_t O_QF = 7 * XB, O_WOQ = O_QF + 663552, O_WOF = O_WOQ + 663552, O_WF = O_WOF + 663552;
constexpr size_t O_QTF = O_WF + 2064384, O_SS0 = O_QTF + 884736, O_SS2 = O_SS0 + 8192, O_G = O_SS2 + 6 * 8192;
constexpr int GP = 3072;
constexpr size_t GL = (size_t)256 * GP;

DI f32x4 mfma(bf16x8 a, bf16x8 b, f32x4 c) { return __builtin_amdgcn_mfma_f32_16x16x32_bf16(a, b, c, 0, 0, 0); }
DI unsigned pk2(float a, float b) { f32x2 f = {a, b}; return __builtin_bit_cast(unsigned, __builtin_convertvector(f, bf16x2)); }
DI bf16x8 pk8(f32x4 a, f32x4 b) { u32x4 u = {pk2(a[0], a[1]), pk2(a[2], a[3]), pk2(b[0], b[1]), pk2(b[2], b[3])}; return __builtin_bit_cast(bf16x8, u); }
DI float lo16(unsigned u) { return __builtin_bit_cast(float, u << 16); }
DI float hi16(unsigned u) { return __builtin_bit_cast(float, u & 0xffff0000u); }
DI float cosr(float rev) { return __builtin_amdgcn_cosf(__builtin_amdgcn_fractf(rev)); }
DI float sinr(float rev) { return __builtin_amdgcn_sinf(__builtin_amdgcn_fractf(rev)); }
DI void glds(const char* g, char* l) { __builtin_amdgcn_global_load_lds((const GLBP void*)g, (LDSP void*)l, 16, 0, 0); }
DI bf16x8 ldf(const char* p) { return *(const bf16x8*)p; }
typedef __amdgpu_buffer_rsrc_t rsrc_t;
DI rsrc_t mkrs(const void* p, unsigned bytes) { return __builtin_amdgcn_make_buffer_rsrc((void*)p, 0, bytes, 0x00020000); }
DI void bglds(rsrc_t r, char* l, int voff, int soff) { __builtin_amdgcn_raw_ptr_buffer_load_lds(r, (LDSP void*)l, 16, voff, soff, 0, 0); }
#define ALD16(dst, voff, sbase, imm) asm volatile("global_load_dwordx4 %0, %1, %2 offset:" #imm : "=v"(dst) : "v"(voff), "s"(sbase))
#define ALD8(dst, voff, sbase) asm volatile("global_load_dwordx2 %0, %1, %2" : "=v"(dst) : "v"(voff), "s"(sbase))
#define PIN(x) asm volatile("" : "+v"(x))
DI bf16x8 trf(const char* p, int d) {
  s16x4 lo = __builtin_amdgcn_ds_read_tr16_b64_v4i16((LDSP s16x4*)p), hi = __builtin_amdgcn_ds_read_tr16_b64_v4i16((LDSP s16x4*)(p + d));
  return __builtin_bit_cast(bf16x8, __builtin_shufflevector(lo, hi, 0, 1, 2, 3, 4, 5, 6, 7));
}
DI u32x2 pk4(f32x4 v) { return (u32x2){pk2(v[0], v[1]), pk2(v[2], v[3])}; }
constexpr u32x4 ONES = {0x3f803f80u, 0x3f803f80u, 0x3f803f80u, 0x3f803f80u};
constexpr f32x4 Z4 = {0.f, 0.f, 0.f, 0.f};
DI float qent(int d, int j) {
  int f = (j >> 1) + 1; float ph = (j & 1) ? 0.25f : 0.f, sc = 0.05892556509887896f;
  if (j >= 574) { sc = 0.041666666666666664f; ph = 0.f; f = (j == 575) ? 0 : 288; }
  return sc * cosr((float)(f * d) * (1.f / 576.f) + ph);
}
DI int operm(int k) {
  int hp = k / 36, c = k - 36 * hp, p = c >> 2, i = c & 3;
  return p < 8 ? 18 * (2 * hp + (p >> 2)) + 4 * (p & 3) + i : 18 * (2 * hp + (i >> 1)) + 16 + (i & 1);
}

template <int MODE>
DI void gemm_q(char* sm, int rb, int ng, const char* xs, char* out, float* ssq, char* qf_out) {
  const int tid = threadIdx.x, w = __builtin_amdgcn_readfirstlane(tid >> 6), l = tid & 63, g = l >> 4, m = l & 15;
  const int kh = w >= 6, nt = w - 6 * kh, jt = 6 * ng + nt;
  STAMP(0, 0);
  {
    const rsrc_t xrs = mkrs(xs, MODE == 0 ? (unsigned)XB : 663552u);
    int voff[2];
#pragma unroll
    for (int h = 0; h < 2; ++h) {
      int r = 64 * rb + 8 * ((w + 4 * h) & 7) + (l >> 3);
      voff[h] = (MODE == 1 ? operm(r) : r) * 1152 + (((l & 7) ^ (l >> 3)) << 4);
    }
#pragma unroll
    for (int i = 0; i < 6; ++i) { int q = w + 12 * i; bglds(xrs, sm + q * 1024, voff[i & 1], (q >> 3) * 128); }
  }
  SB(); STAMP(0, 1);
  bf16x8 qf[9];
  {
    const int j = 16 * jt + m;
    int f = (j >> 1) + 1; float ph = (j & 1) ? 0.25f : 0.f, sc = 0.05892556509887896f;
    if (j >= 574) { sc = 0.041666666666666664f; ph = 0.f; f = (j == 575) ? 0 : 288; }
    const float st = (float)f * (1.f / 576.f), c2 = 2.f * __builtin_amdgcn_cosf(st), p0 = st * (float)(8 * g + 32 * kh) + ph;
#pragma unroll
    for (int i = 0; i < 9; ++i) {
      float p = __builtin_amdgcn_fractf(st * (float)(64 * i) + p0), v[8];
      v[0] = sc * __builtin_amdgcn_cosf(p); v[1] = sc * __builtin_amdgcn_cosf(p + st);
#pragma unroll
      for (int e = 2; e < 8; ++e) v[e] = c2 * v[e - 1] - v[e - 2];
      qf[i] = __builtin_bit_cast(bf16x8, (u32x4){pk2(v[0], v[1]), pk2(v[2], v[3]), pk2(v[4], v[5]), pk2(v[6], v[7])});
      PIN(qf[i]);
    }
  }
  WAITV(0); BAR(); STAMP(0, 2);
  if (MODE == 0 && rb == 0) {
#pragma unroll
    for (int i = 0; i < 9; ++i) *(bf16x8*)(qf_out + (jt * 18 + 2 * i + kh) * 1024 + l * 16) = qf[i];
  }
  f32x4 acc[4] = {}, gr = Z4, su = Z4;
  const bool stats = MODE == 0 && ng == 0 && nt < 4;
  const int x2 = 2 * kh;
  const char* xb = sm + (m >> 3) * 1024 + (m & 7) * 128 + (((4 * kh + g) ^ (m & 7)) << 4);
#pragma unroll
  for (int i = 0; i < 9; ++i) {
#pragma unroll
    for (int a = 0; a < 4; ++a) {
      bf16x8 xf = ldf(xb + i * 8192 + (a ^ x2) * 2048);
      acc[a] = MODE == 1 ? mfma(xf, qf[i], acc[a]) : mfma(qf[i], xf, acc[a]);
    }
    if (stats) { bf16x8 xf = ldf(xb + i * 8192 + nt * 2048); gr = mfma(xf, xf, gr); su = mfma(__builtin_bit_cast(bf16x8, ONES), xf, su); }
  }
  BAR(); STAMP(0, 4);
  *(f32x4*)(sm + w * 4096 + l * 16) = acc[2]; *(f32x4*)(sm + w * 4096 + 1024 + l * 16) = acc[3];
  if (stats && kh) { *(f32x4*)(sm + w * 4096 + 2048 + l * 16) = gr; *(f32x4*)(sm + w * 4096 + 3072 + l * 16) = su; }
  BAR();
  const char* pp = sm + (kh ? w - 6 : w + 6) * 4096 + l * 16;
#pragma unroll
  for (int a = 0; a < 2; ++a) {
    f32x4 v = acc[a] + *(const f32x4*)(pp + a * 1024);
    const int rt = 4 * rb + x2 + a, n = 16 * jt + 4 * g;
    if (MODE == 1) *(u32x2*)(out + (jt * 18 + (rt >> 1)) * 1024 + (m + 16 * (2 * (rt & 1) + (g >> 1))) * 16 + (g & 1) * 8) = pk4(v);
    else *(u32x2*)(out + ((n >> 3) * S + 16 * rt + m) * 16 + (n & 4) * 2) = pk4(v);
  }
  if (stats && !kh) {
    gr += *(const f32x4*)(pp + 2048); su += *(const f32x4*)(pp + 3072);
    float dsel = (m & 2) ? ((m & 1) ? gr[3] : gr[2]) : ((m & 1) ? gr[1] : gr[0]);
    if ((m >> 2) == g) ssq[64 * rb + 16 * nt + m] = fmaxf(dsel - su[0] * su[0] * (1.f / 576.f), 0.f);
  }
  FENCE(); STAMP(0, 5);
}

__global__ void __launch_bounds__(768, 6) k_pre(const char* x0, const char* wo, const char* wr, const char* wi, char* ws) {
  extern __shared__ __attribute__((aligned(16))) char sm[];
  const int b = blockIdx.x, tid = threadIdx.x;
  if (b < 192) {
    int x = b & 7, i = b >> 3;
    gemm_q<0>(sm, 4 * x + (i & 3), i >> 2, x0, ws + O_XH0, (float*)(ws + O_SS0), ws + O_QF);
  } else if (b < 246) {
    int u = b - 192;
    gemm_q<1>(sm, u % 9, u / 9, wo, ws + O_WOQ, nullptr, nullptr);
  } else {
    STAMP(0, 0);
    int u = b - 246, tg = u >> 3, kb = 8 * (u & 7);
    u32x4 v[4];
#pragma unroll
    for (int i = 0; i < 4; ++i) {
      int e = i * 768 + tid; e = e < 2312 ? e : 2311;
      int run = e / 289, o = e - 289 * run;
      v[i] = *(const u32x4*)((run < 4 ? wr : wi) + (kb + 64 * (4 * tg + (run & 3))) * 578 + o * 16);
    }
    {
      const int id = u * 768 + tid;
      *(u32x4*)(ws + O_G + id * 16) = (u32x4){0u, 0u, 0u, 0u};
      if (id < 4608) *(u32x4*)(ws + O_WF + ((7 * (id / 288) + 6) * 18 + (id % 288 >> 4)) * 1024 + (12 + (id & 3) + 16 * ((id >> 2) & 3)) * 16) = (u32x4){0u, 0u, 0u, 0u};
    }
#pragma unroll
    for (int i = 0; i < 4; ++i) { int e = i * 768 + tid; e = e < 2312 ? e : 2311; *(u32x4*)(sm + e * 16) = v[i]; }
    __syncthreads();
    STAMP(0, 2);
#pragma unroll
    for (int i = 0; i < 3; ++i) {
      int e = i * 768 + tid, mm = e & 7, c = e >> 3;
      const char* p = sm + mm * 578 + 2 * (c + 1);
      u32x4 o;
#pragma unroll
      for (int r = 0; r < 4; ++r) o[r] = (unsigned)*(const u16*)(p + r * 4624) | ((unsigned)*(const u16*)(p + (4 + r) * 4624) << 16);
      *(u32x4*)(ws + O_WT + ((c * 4 + (kb >> 4)) * 2 + (tg >> 2)) * 1024 + (16 * (tg & 3) + (kb & 8) + mm) * 16) = o;
    }
    FENCE(); STAMP(0, 5);
  }
}

template <int NP>
DI void fft_cols(char* sm, int b, const char* xin, char* xout, const float* ssq, const char* wt) {
  const int tid = threadIdx.x, w8 = __builtin_amdgcn_readfirstlane(tid >> 6), l = tid & 63, g = l >> 4, m = l & 15;
  const int w = w8 & 3, hi = w8 >> 2;
  const int kb = (b & 7) + 8 * (b >> 4), half = (b >> 3) & 1;
  const bool last = kb == 71 && half == 1;
  constexpr int KID = NP == 1 ? 1 : 4;
  STAMP(KID, 0);
  f32x4 ps[NP];
#pragma unroll
  for (int p = 0; p < NP; ++p) ps[p] = *(const f32x4*)(ssq + p * S + tid * 4);
  u32x2 xv[4];
  const int xo = (kb * S + 32 * (16 * w + 4 * g) + 16 * hi + m) * 16 + 8 * half;
#pragma unroll
  for (int r = 0; r < 4; ++r) xv[r] = *(const u32x2*)(xin + xo + r * 512);
  u32x4 wv[2];
#pragma unroll
  for (int t = 0; t < 2; ++t) wv[t] = *(const u32x4*)(wt + (((4 * kb + 2 * half + hi) * 4 + w) * 2 + t) * 1024 + l * 16);
  FENCE(); STAMP(KID, 1);
  SB();
  bf16x8 c64[2], s64[2], n64[2], c32[2], s32[2], n32[2];
  f32x4 t1c[2], t1s[2], t2c[2], t2s[2];
  {
    const float k1 = (float)(16 * w + m), g4 = (float)(4 * g);
    const unsigned sg = (m & 1) ? 0x80008000u : 0u;
    float c[8], sn[8];
    const float a = k1 * (1.f / 64.f), a2 = 2.f * __builtin_amdgcn_cosf(a), p = a * (g4 + g4);
    c[0] = __builtin_amdgcn_cosf(p); c[1] = __builtin_amdgcn_cosf(p + a); sn[0] = __builtin_amdgcn_sinf(p); sn[1] = __builtin_amdgcn_sinf(p + a);
#pragma unroll
    for (int j = 2; j < 8; ++j) { c[j] = a2 * c[j - 1] - c[j - 2]; sn[j] = a2 * sn[j - 1] - sn[j - 2]; }
    u32x4 uc = {pk2(c[0], c[1]), pk2(c[2], c[3]), pk2(c[4], c[5]), pk2(c[6], c[7])}, us = {pk2(sn[0], sn[1]), pk2(sn[2], sn[3]), pk2(sn[4], sn[5]), pk2(sn[6], sn[7])};
    c64[0] = __builtin_bit_cast(bf16x8, uc); c64[1] = __builtin_bit_cast(bf16x8, uc ^ sg);
    s64[0] = __builtin_bit_cast(bf16x8, us); s64[1] = __builtin_bit_cast(bf16x8, us ^ sg);
    n64[0] = __builtin_bit_cast(bf16x8, us ^ 0x80008000u); n64[1] = __builtin_bit_cast(bf16x8, us ^ (sg ^ 0x80008000u));
    const float e = (float)m * (1.f / 32.f);
#pragma unroll
    for (int j = 0; j < 4; ++j) { float q = e * (g4 + (float)j); c[j] = __builtin_amdgcn_cosf(q); sn[j] = __builtin_amdgcn_sinf(q); }
    unsigned c0 = pk2(c[0], c[1]), c1 = pk2(c[2], c[3]), s0 = pk2(sn[0], sn[1]), s1 = pk2(sn[2], sn[3]);
    uc = (u32x4){c0, c1, c0 ^ sg, c1 ^ sg}; us = (u32x4){s0, s1, s0 ^ sg, s1 ^ sg};
    c32[0] = __builtin_bit_cast(bf16x8, uc); c32[1] = __builtin_bit_cast(bf16x8, uc ^ 0x80000000u);
    s32[0] = __builtin_bit_cast(bf16x8, us); s32[1] = __builtin_bit_cast(bf16x8, us ^ 0x80000000u);
    n32[0] = __builtin_bit_cast(bf16x8, us ^ 0x80008000u); n32[1] = __builtin_bit_cast(bf16x8, us ^ 0x00008000u);
    const float b1 = k1 * (1.f / 2048.f), b12 = 2.f * __builtin_amdgcn_cosf(b1);
#pragma unroll
    for (int h = 0; h < 2; ++h) {
      const float p1 = b1 * (g4 + (float)(16 * h)), b2 = (float)(16 * h + m) * (1.f / 2048.f), b22 = 2.f * __builtin_amdgcn_cosf(b2), p2 = b2 * (g4 + (float)(16 * w));
      t1c[h][0] = __builtin_amdgcn_cosf(p1); t1c[h][1] = __builtin_amdgcn_cosf(p1 + b1); t1s[h][0] = __builtin_amdgcn_sinf(p1); t1s[h][1] = __builtin_amdgcn_sinf(p1 + b1);
      t2c[h][0] = __builtin_amdgcn_cosf(p2); t2c[h][1] = __builtin_amdgcn_cosf(p2 + b2); t2s[h][0] = __builtin_amdgcn_sinf(p2); t2s[h][1] = __builtin_amdgcn_sinf(p2 + b2);
#pragma unroll
      for (int r = 2; r < 4; ++r) {
        t1c[h][r] = b12 * t1c[h][r - 1] - t1c[h][r - 2]; t1s[h][r] = b12 * t1s[h][r - 1] - t1s[h][r - 2];
        t2c[h][r] = b22 * t2c[h][r - 1] - t2c[h][r - 2]; t2s[h][r] = b22 * t2s[h][r - 1] - t2s[h][r - 2];
      }
      PIN(c64[h]); PIN(s64[h]); PIN(n64[h]); PIN(c32[h]); PIN(s32[h]); PIN(n32[h]); PIN(t1c[h]); PIN(t1s[h]); PIN(t2c[h]); PIN(t2s[h]);
    }
  }
  SB();
  FENCE(); STAMP(KID, 2);
  float* rsl = (float*)(sm + 32768);
  {
    f32x4 a = ps[0];
#pragma unroll
    for (int p = 1; p < NP; ++p) a += ps[p];
#pragma unroll
    for (int i = 0; i < 4; ++i) a[i] = __builtin_amdgcn_rsqf(a[i] * (1.f / 576.f) + 1e-5f) * (1.f / 2048.f);
    *(f32x4*)(rsl + tid * 4) = a;
  }
  BAR();
  const int wo = m * 128 + (((2 * w + (g >> 1)) ^ (m & 7)) << 4) + (g & 1) * 8;
  {
    f32x4 u[4];
#pragma unroll
    for (int r = 0; r < 4; ++r) {
      float rs = rsl[32 * (16 * w + 4 * g + r) + 16 * hi + m];
      u[0][r] = lo16(xv[r][0]) * rs; u[1][r] = hi16(xv[r][0]) * rs; u[2][r] = lo16(xv[r][1]) * rs;
      u[3][r] = last ? 0.f : hi16(xv[r][1]) * rs;
    }
#pragma unroll
    for (int p = 0; p < 4; ++p) *(u32x2*)(sm + p * 4096 + hi * 2048 + wo) = pk4(u[p]);
  }
  BAR(); STAMP(KID, 3);
  const int ro0 = m * 128 + ((g ^ (m & 7)) << 4), ro1 = ro0 ^ 64;
  {
    f32x4 re[2], im[2];
#pragma unroll
    for (int t = 0; t < 2; ++t) {
      const char* p = sm + hi * 8192 + t * 2048;
      bf16x8 a0 = ldf(p + ro0), a1 = ldf(p + ro1), b0 = ldf(p + 4096 + ro0), b1 = ldf(p + 4096 + ro1);
      f32x4 x = mfma(a0, c64[0], Z4), y = mfma(b0, c64[0], Z4);
      x = mfma(a1, c64[1], x); y = mfma(b1, c64[1], y);
      x = mfma(b0, s64[0], x); y = mfma(a0, n64[0], y);
      x = mfma(b1, s64[1], x); y = mfma(a1, n64[1], y);
#pragma unroll
      for (int r = 0; r < 4; ++r) { re[t][r] = x[r] * t1c[t][r] + y[r] * t1s[t][r]; im[t][r] = y[r] * t1c[t][r] - x[r] * t1s[t][r]; }
    }
    bf16x8 fr = pk8(re[0], re[1]), fi = pk8(im[0], im[1]);
#pragma unroll
    for (int t = 0; t < 2; ++t) {
      f32x4 x = mfma(c32[t], fr, Z4), y = mfma(c32[t], fi, Z4);
      x = mfma(s32[t], fi, x); y = mfma(n32[t], fr, y);
#pragma unroll
      for (int r = 0; r < 4; ++r) {
        float a = lo16(wv[t][r]), bb = hi16(wv[t][r]);
        re[t][r] = x[r] * a - y[r] * bb; im[t][r] = x[r] * bb + y[r] * a;
      }
    }
    fr = pk8(re[0], re[1]); fi = pk8(im[0], im[1]);
#pragma unroll
    for (int t = 0; t < 2; ++t) {
      f32x4 x = mfma(fr, c32[t], Z4), y = mfma(fi, c32[t], Z4);
      x = mfma(fi, n32[t], x); y = mfma(fr, s32[t], y);
      f32x4 vr, vi;
#pragma unroll
      for (int r = 0; r < 4; ++r) { vr[r] = x[r] * t2c[t][r] - y[r] * t2s[t][r]; vi[r] = y[r] * t2c[t][r] + x[r] * t2s[t][r]; }
      char* p = sm + 16384 + hi * 8192 + t * 2048 + wo;
      *(u32x2*)p = pk4(vr); *(u32x2*)(p + 4096) = pk4(vi);
    }
  }
  BAR(); STAMP(KID, 4);
  {
    f32x4 yr[2], yi[2];
#pragma unroll
    for (int c = 0; c < 2; ++c) {
      const char* p = sm + 16384 + c * 8192 + hi * 2048;
      bf16x8 a0 = ldf(p + ro0), a1 = ldf(p + ro1), b0 = ldf(p + 4096 + ro0), b1 = ldf(p + 4096 + ro1);
      f32x4 x = mfma(c64[0], a0, Z4), y = mfma(c64[0], b0, Z4);
      x = mfma(c64[1], a1, x); y = mfma(c64[1], b1, y);
      x = mfma(n64[0], b0, x); y = mfma(s64[0], a0, y);
      x = mfma(n64[1], b1, x); y = mfma(s64[1], a1, y);
      yr[c] = x; yi[c] = y;
    }
#pragma unroll
    for (int r = 0; r < 4; ++r) {
      u32x2 o = {pk2(lo16(xv[r][0]) + yr[0][r], hi16(xv[r][0]) + yi[0][r]),
                 pk2(lo16(xv[r][1]) + yr[1][r], hi16(xv[r][1]) + (last ? 0.f : yi[1][r]))};
      *(u32x2*)(xout + xo + r * 512) = o;
    }
  }
  FENCE(); STAMP(KID, 5);
}

DI void wqkv_rot(char* sm, int u, const char* wqkv, const char* qf, char* wf) {
  const int tid = threadIdx.x, w = __builtin_amdgcn_readfirstlane(tid >> 6), l = tid & 63, g = l >> 4, m = l & 15;
  const int jg = u % 5, ng = u / 5, kh = w >> 2, jt0 = 8 * jg + 2 * (w & 3);
  const bool act = jt0 < 36;
  STAMP(1, 0);
  const rsrc_t rs = mkrs(wqkv, 1990656u);
  const int voff = (l >> 3) * 3456 + (((((l >> 1) & 3) ^ (((l >> 4) & 1) | ((w & 1) << 1))) << 5) | ((l & 1) << 4));
  bf16x8 af[2][9];
  const char* qs = qf + ((act ? jt0 : 0) * 18 + 9 * kh) * 1024;
  const char *q0 = qs + kh * 18432, *q1 = qs + (1 - kh) * 18432;
  const int l16 = l * 16;
#pragma unroll
  for (int i = 0; i < 9; ++i) {
    bglds(rs, sm + (w + 8 * i) * 1024, voff, (w + 8 * i) * 27648 + ng * 128);
    const char *a0 = q0 + i * 1024, *a1 = q1 + i * 1024;
    ALD16(af[0][i], l16, a0, 0); ALD16(af[1][i], l16, a1, 0);
  }
  int ob[4];
#pragma unroll
  for (int t = 0; t < 4; ++t) {
    int n = 64 * ng + 16 * t + m, seg = (n >= 576) + (n >= 1152), c = n - 576 * seg, hp = c / 36;
    int cc = c - 36 * hp + (seg == 0 ? 72 : seg == 1 ? 0 : 36);
    ob[t] = (7 * hp + (cc >> 4)) * 18432 + (cc & 15) * 16;
  }
  STAMP(1, 2);
  WAITV(0); BAR(); STAMP(1, 3);
  f32x4 acc[2][4] = {};
  const int rbase = 9 * kh * 4096 + (8 * g + (m >> 2)) * 128 + 8 * (m & 3), fx = ((m >> 3) & 1) | ((g & 1) << 1);
#pragma unroll
  for (int i = 0; i < 9; ++i) {
    PIN(af[0][i]); PIN(af[1][i]);
#pragma unroll
    for (int t = 0; t < 4; ++t) {
      bf16x8 bfr = trf(sm + i * 4096 + rbase + ((t ^ fx) << 5), 512);
      acc[0][t] = mfma(af[0][i], bfr, acc[0][t]); acc[1][t] = mfma(af[1][i], bfr, acc[1][t]);
    }
  }
  BAR(); STAMP(1, 4);
#pragma unroll
  for (int t = 0; t < 4; ++t) *(f32x4*)(sm + (w * 4 + t) * 1024 + l16) = acc[1][t];
  BAR();
  const int jt = jt0 + kh, jo = (jt >> 1) * 1024 + (2 * (jt & 1) + (g >> 1)) * 256 + (g & 1) * 8;
#pragma unroll
  for (int t = 0; t < 4; ++t) {
    f32x4 o = acc[0][t] + *(const f32x4*)(sm + ((w ^ 4) * 4 + t) * 1024 + l16);
    if (jt == 35 && g == 3) o[3] = 0.f;
    if (act) *(u32x2*)(wf + ob[t] + jo) = pk4(o);
  }
  FENCE(); STAMP(1, 5);
}

__global__ void __launch_bounds__(512, 2) k_fft1(const char* wqkv, char* ws) {
  extern __shared__ __attribute__((aligned(16))) char sm[];
  const int b = blockIdx.x;
  if (b < 144) fft_cols<1>(sm, b, ws + O_XH0, ws + O_XH1, (const float*)(ws + O_SS0), ws + O_WT);
  else wqkv_rot(sm, b - 144, wqkv, ws + O_QF, ws + O_WF);
}
__global__ void __launch_bounds__(512, 2) k_fft2(const char* wo, char* ws) {
  extern __shared__ __attribute__((aligned(16))) char sm[];
  const int b = blockIdx.x;
  if (b < 144) fft_cols<6>(sm, b, ws + O_XH2, ws + O_XH3, (const float*)(ws + O_SS2), ws + O_WT);
  else {
    const int e = (b - 144) * 512 + threadIdx.x, l = e & 63, pc = e >> 6, nt = pc / 18, ks = pc - 18 * nt;
    unsigned h[8];
    if (e < 41472) {
      const int r0 = operm(32 * ks + 8 * (l >> 4)), r1 = operm(32 * ks + 8 * (l >> 4) + 4), c0 = (32 * ks + 8 * (l >> 4)) % 36;
#pragma unroll
      for (int j = 0; j < 8; ++j) {
        int r = (j < 4 ? r0 : r1) + (c0 == (j < 4 ? 32 : 28) ? 18 * ((j >> 1) & 1) + (j & 1) : (j & 3));
        h[j] = *(const u16*)(wo + r * 1152 + (16 * nt + (l & 15)) * 2);
      }
    }
    if (e < (int)(GL / 16)) *(u32x4*)(ws + O_G + GL + e * 16) = (u32x4){0u, 0u, 0u, 0u};
    const int hp = nt / 3, c = 16 * (nt - 3 * hp) + (l & 15), d = 36 * hp + (c < 36 ? c : 0), j0 = 32 * ks + 8 * (l >> 4);
    *(u32x4*)(ws + O_QTF + e * 16) = (u32x4){pk2(qent(d, j0), qent(d, j0 + 1)), pk2(qent(d, j0 + 2), qent(d, j0 + 3)),
                                             pk2(qent(d, j0 + 4), qent(d, j0 + 5)), pk2(qent(d, j0 + 6), qent(d, j0 + 7))};
    if (e < 41472) *(u32x4*)(ws + O_WOF + e * 16) = (u32x4){h[0] | (h[1] << 16), h[2] | (h[3] << 16), h[4] | (h[5] << 16), h[6] | (h[7] << 16)};
  }
}

constexpr int A_KV = 129024;
constexpr int A_PK = 65536, A_V = A_PK + 13056, A_Q = A_V + 13056;
template <int LAST>
__global__ void __launch_bounds__(512, 2) k_attn(const char* xin, char* ws) {
  extern __shared__ __attribute__((aligned(16))) char sm[];
  const int tid = threadIdx.x, w = __builtin_amdgcn_readfirstlane(tid >> 6), l = tid & 63, g = l >> 4, m = l & 15;
  const int b = blockIdx.x, rb = 4 * ((b & 7) >> 1) + ((b >> 3) & 3), hp = 8 * (b & 1) + (b >> 5);
  const int wr = w & 3, kh = w >> 2, s0 = 128 * rb + 32 * wr;
  constexpr int KID = LAST ? 5 : 2;
  STAMP(KID, 0);
  const rsrc_t wrs = mkrs(ws + O_WF + hp * 129024, 129024);
  const int l16 = l * 16, xo0 = (g * S + s0 + 16 * kh + m) * 16, xo1 = xo0 ^ 256;
  bf16x8 xf[2][9];
  auto issue = [&](int j) {
#pragma unroll
    for (int i = 0; i < 2; ++i) {
      int q = w + 8 * i; q = q < 14 ? q : q - 8;
      int pc = (q >> 1) * 18 + 2 * j + (q & 1);
      bglds(wrs, sm + pc * 1024, l16, pc * 1024);
    }
    const char* xb = xin + (2 * j + kh) * (4 * S * 16);
    ALD16(xf[0][j], xo0, xb, 0); ALD16(xf[1][j], xo1, xb, 0);
    FENCE();
  };
#pragma unroll
  for (int j = 0; j < 4; ++j) issue(j);
  if (tid < 256) *(u32x4*)(sm + A_KV + tid * 16) = (u32x4){0u, 0u, 0u, 0u};
  const int pa = w < 6 ? w / 3 : 2, pb = w < 6 ? w - 3 * pa : w - 5;
  const int dq = 4 * pa + g, ec = 16 * pb + m - 4, eh = ec >= 18;
  const bool isz = ec == 36, valid = isz ? dq < 9 : (ec >= 0 && ec < 36 && dq >= 4 * eh && dq <= 4 * eh + 4);
  const int slot = (isz ? 180 + dq : 36 * (dq - 4 * eh) + ec) * 16;
  STAMP(KID, 1);
  f32x4 acc[2][8] = {};
#pragma unroll
  for (int i = 0; i < 9; ++i) {
    if (i <= 5) WAITV(12); else if (i == 6) WAITV(8); else if (i == 7) WAITV(4); else WAITV(0);
    BAR();
    if (i == 0) STAMP(KID, 2);
    bf16x8 wf[7];
#pragma unroll
    for (int t = 0; t < 7; ++t) wf[t] = ldf(sm + (t * 18 + 2 * i + kh) * 1024 + l16);
    if (i + 4 < 9) issue(i + 4);
    PIN(xf[0][i]); PIN(xf[1][i]);
#pragma unroll
    for (int a = 0; a < 2; ++a) {
      bf16x8 xm = xf[a][i];
      if (i == 8) { u32x4 u = __builtin_bit_cast(u32x4, xm); u[3] = (g == 3 && kh) ? (u[3] & 0xffffu) : u[3]; xm = __builtin_bit_cast(bf16x8, u); }
      acc[a][7] = mfma(xm, xm, acc[a][7]);
    }
#pragma unroll
    for (int t = 0; t < 7; ++t) { acc[0][t] = mfma(xf[0][i], wf[t], acc[0][t]); acc[1][t] = mfma(xf[1][i], wf[t], acc[1][t]); }
  }
  BAR(); STAMP(KID, 3);
#pragma unroll
  for (int t = 0; t < 8; ++t) *(f32x4*)(sm + (w * 8 + t) * 1024 + l16) = acc[1][t];
  BAR();
  f32x4 v[8];
#pragma unroll
  for (int t = 0; t < 8; ++t) v[t] = acc[0][t] + *(const f32x4*)(sm + ((w ^ 4) * 8 + t) * 1024 + l16);
  {
    f32x4 rs, rl;
#pragma unroll
    for (int r = 0; r < 4; ++r) { rs[r] = __builtin_amdgcn_rsqf(__shfl(v[7][r], 20 * g + r) * (1.f / 576.f) + 1e-5f); rl[r] = rs[r] * 1.4426950408889634f; }
    const int so = (32 * wr + 16 * kh + 4 * g) * 2 + m * 272;
#pragma unroll
    for (int t = 0; t < 7; ++t) {
      if (t != 3) {
        f32x4 p;
#pragma unroll
        for (int r = 0; r < 4; ++r) p[r] = fmaxf(v[t][r] * rs[r] + 1e-6f, 1e-6f) + __builtin_amdgcn_exp2f(fminf(v[t][r] * rl[r], 0.f));
        if (t < 3) *(u32x2*)(sm + A_PK + t * 4352 + so) = pk4(p);
        else *(u32x2*)(sm + A_Q + w * 1536 + (16 * (t - 4) + m) * 32 + g * 8) = pk4(p);
      }
      if (t >= 2 && t <= 4) {
        u32x2 u = pk4(v[t] * rs);
        if (t == 4 && m == 8) u = (u32x2){0x3f803f80u, 0x3f803f80u};
        *(u32x2*)(sm + A_V + (t - 2) * 4352 + so) = u;
      }
    }
  }
  BAR(); STAMP(KID, 4);
  const rsrc_t grs = mkrs(ws + O_G + (LAST ? GL : 0) + hp * 16 * GP, 16 * GP);
  if (LAST) {
    const rsrc_t qrs = mkrs(ws + O_QTF + hp * 55296, 55296);
#pragma unroll
    for (int i = 0; i < 7; ++i) { int q = w + 8 * i; q = q < 54 ? q : q - 8; bglds(qrs, sm + q * 1024, l16, q * 1024); }
  }
  {
    f32x4 c = Z4;
#pragma unroll
    for (int k = 0; k < 4; ++k)
      c = mfma(ldf(sm + A_PK + pa * 4352 + m * 272 + k * 64 + g * 16), ldf(sm + A_V + pb * 4352 + m * 272 + k * 64 + g * 16), c);
    u32x4 u = ((__builtin_bit_cast(u32x4, c) + 0x80u) & 0xffffff00u) | 0x5au;
    if (valid) __builtin_amdgcn_raw_buffer_store_b128(u, grs, slot + rb * GP, 0, 16);
  }
  FENCE(); STAMP(KID, 5);
  if (LAST) {
    WAITV(1); BAR();
    f32x4 y[2][3] = {};
#pragma unroll
    for (int i = 0; i < 9; ++i)
#pragma unroll
      for (int t = 0; t < 3; ++t) {
        bf16x8 qf = ldf(sm + (t * 18 + 2 * i + kh) * 1024 + l16);
        y[0][t] = mfma(qf, xf[0][i], y[0][t]); y[1][t] = mfma(qf, xf[1][i], y[1][t]);
      }
    char* ex = sm + A_PK;
#pragma unroll
    for (int t = 0; t < 3; ++t) *(f32x4*)(ex + (w * 3 + t) * 1024 + l16) = y[1][t];
    BAR();
#pragma unroll
    for (int t = 0; t < 3; ++t) {
      f32x4 o = y[0][t] + *(const f32x4*)(ex + ((w ^ 4) * 3 + t) * 1024 + l16);
      if (t < 2 || g == 0) *(u32x2*)(ws + O_X3 + (s0 + 16 * kh + m) * 1152 + (36 * hp + 16 * t + 4 * g) * 2) = pk4(o);
    }
  }
  FENCE(); STAMP(KID, 6);
  {
    u32x4 r[16];
    for (int spin = 0; spin < 100000; ++spin) {
      FENCE();
      unsigned ok = 0xffu;
      if (valid) {
#pragma unroll
        for (int p = 0; p < 16; ++p) r[p] = __builtin_amdgcn_raw_buffer_load_b128(grs, slot, p * GP, 16);
#pragma unroll
        for (int p = 0; p < 16; ++p) ok &= (r[p][0] & r[p][1]) & (r[p][2] & r[p][3]);
      } else ok = 0x5au;
      if (__all(ok == 0x5au)) break;
      __builtin_amdgcn_s_sleep(1);
    }
    if (valid) {
      f32x4 sum = Z4;
#pragma unroll
      for (int p = 0; p < 16; ++p) sum += __builtin_bit_cast(f32x4, r[p] & 0xffffff00u);
      u32x2 u = pk4(sum);
      if (isz ? dq <= 4 : !eh) *(u32x2*)(sm + A_KV + (isz ? 18 : ec) * 64 + 16 + 8 * dq) = (u32x2){u[0], dq == 4 ? 0u : u[1]};
      if (isz ? dq >= 4 : eh) *(u32x2*)(sm + A_KV + (isz ? 50 : ec + 14) * 64 - 16 + 8 * dq) = (u32x2){dq == 4 ? 0u : u[0], u[1]};
    }
  }
  BAR(); STAMP(KID, 7);
  {
    unsigned tail[2];
    char* ob = ws + O_OB + (s0 + 16 * kh + m) * 16;
#pragma unroll
    for (int h = 0; h < 2; ++h) {
      bf16x8 k0 = ldf(sm + A_KV + (32 * h + m) * 64 + g * 16), k1 = ldf(sm + A_KV + (32 * h + 16 + m) * 64 + g * 16);
      bf16x8 pf = trf(sm + A_Q + w * 1536 + (16 * h + 8 * g + (m >> 2)) * 32 + (m & 3) * 8, 128);
      f32x4 n0 = mfma(k0, pf, Z4), n1 = mfma(k1, pf, Z4);
      float inv = __builtin_amdgcn_rcpf(__shfl(n1[2], m) + 1e-6f);
      int P = 9 * hp + 4 * h + g;
      *(u32x2*)(ob + (P >> 1) * (S * 16) + (P & 1) * 8) = pk4(n0 * inv);
      tail[h] = pk2(n1[0] * inv, n1[1] * inv);
    }
    int P = 9 * hp + 8;
    if (g == 0) *(u32x2*)(ob + (P >> 1) * (S * 16) + (P & 1) * 8) = (u32x2){tail[0], tail[1]};
  }
  FENCE(); STAMP(KID, 8);
}

template <int MODE>
__global__ void __launch_bounds__(512, 2) k_out(char* ws, char* outp) {
  extern __shared__ __attribute__((aligned(16))) char sm[];
  const int tid = threadIdx.x, w = __builtin_amdgcn_readfirstlane(tid >> 6), l = tid & 63, g = l >> 4, m = l & 15;
  const int b = blockIdx.x, rb = 8 * ((b & 7) >> 1) + ((b >> 3) & 7), ng = 3 * (b & 1) + (b >> 6);
  const int wr = w & 3, kh = w >> 2, s = 64 * rb + 16 * wr + m;
  constexpr int KID = MODE == 2 ? 3 : 6;
  STAMP(KID, 0);
  const rsrc_t wrs = mkrs(ws + (MODE == 2 ? O_WOQ : O_WOF) + ng * 110592, 110592);
  const int l16 = l * 16, xoff = (g * S + s) * 16;
  bf16x8 xf[9];
  auto issue = [&](int st) {
#pragma unroll
    for (int i = 0; i < (st < 4 ? 3 : 2); ++i) {
      int q = w + 8 * i; if (st == 4) q = q < 12 ? q : q - 8;
      int pc = st < 4 ? (q >> 2) * 18 + 4 * st + (q & 3) : (q >> 1) * 18 + 16 + (q & 1);
      bglds(wrs, sm + pc * 1024, l16, pc * 1024);
    }
#pragma unroll
    for (int i = 2 * st; i < (st < 4 ? 2 * st + 2 : 9); ++i) { const char* xb = ws + O_OB + (2 * i + kh) * (4 * S * 16); ALD16(xf[i], xoff, xb, 0); }
    FENCE();
  };
  issue(0); issue(1); issue(2);
  u32x2 rv[3];
  int ro[3];
#pragma unroll
  for (int t = 0; t < 3; ++t) {
    int n = 16 * (6 * ng + 3 * kh + t) + 4 * g;
    ro[t] = MODE == 2 ? ((n >> 3) * S + s) * 16 + (n & 4) * 2 : s * 1152 + n * 2;
  }
  STAMP(KID, 1);
  f32x4 acc[6] = {};
  const char *wa = sm + (54 * kh + kh) * 1024 + l16, *wb = sm + (54 - 54 * kh + kh) * 1024 + l16;
#pragma unroll
  for (int st = 0; st < 5; ++st) {
    if (st < 2) WAITV(10); else if (st == 2) WAITV(11); else if (st == 3) WAITV(6); else WAITV(3);
    BAR();
    if (st == 0) STAMP(KID, 2);
    bf16x8 wf[2][6];
#pragma unroll
    for (int k = 0; k < (st < 4 ? 2 : 1); ++k)
#pragma unroll
      for (int t = 0; t < 6; ++t) wf[k][t] = ldf((t < 3 ? wa : wb) + ((t % 3) * 18 + 4 * st + 2 * k) * 1024);
    if (st < 2) issue(st + 3);
    if (st == 1) {
      const char* rp = ws + (MODE == 2 ? O_XH1 : O_X3);
      ALD8(rv[0], ro[0], rp); ALD8(rv[1], ro[1], rp); ALD8(rv[2], ro[2], rp);
    }
#pragma unroll
    for (int k = 0; k < (st < 4 ? 2 : 1); ++k) {
      PIN(xf[2 * st + k]);
#pragma unroll
      for (int t = 0; t < 6; ++t) acc[t] = mfma(wf[k][t], xf[2 * st + k], acc[t]);
    }
  }
  BAR(); STAMP(KID, 4);
#pragma unroll
  for (int t = 0; t < 3; ++t) *(f32x4*)(sm + (w * 3 + t) * 1024 + l16) = acc[3 + t];
  WAITV(0); BAR();
  char* op = MODE == 2 ? ws + O_XH2 : outp;
  float sq = 0.f;
#pragma unroll
  for (int t = 0; t < 3; ++t) {
    PIN(rv[t]);
    f32x4 v = acc[t] + *(const f32x4*)(sm + ((w ^ 4) * 3 + t) * 1024 + l16);
    v[0] += lo16(rv[t][0]); v[1] += hi16(rv[t][0]); v[2] += lo16(rv[t][1]); v[3] += hi16(rv[t][1]);
    *(u32x2*)(op + ro[t]) = pk4(v);
    if (MODE == 2) sq += v[0] * v[0] + v[1] * v[1] + v[2] * v[2] + ((t == 2 && ng == 5 && kh && g == 3) ? 0.f : v[3] * v[3]);
  }
  if (MODE == 2) {
    sq += __shfl_xor(sq, 16); sq += __shfl_xor(sq, 32);
    float* red = (float*)(sm + 32768);
    if (kh && g == 0) red[wr * 16 + m] = sq;
    BAR();
    if (!kh && g == 0) ((float*)(ws + O_SS2))[ng * S + s] = sq + red[wr * 16 + m];
  }
  FENCE(); STAMP(KID, 5);
}

extern "C" void kernel_launch(void* const* d_in, const int* in_sizes, int n_in, void* d_out, int out_size, void* d_ws,
                              size_t ws_size, hipStream_t stream) {
  const char *x0 = (const char*)d_in[0], *wr = (const char*)d_in[1], *wi = (const char*)d_in[2];
  const char *wqkv = (const char*)d_in[4], *wo = (const char*)d_in[5];
  char* ws = (char*)d_ws;
  (void)hipFuncSetAttribute((const void*)k_pre, hipFuncAttributeMaxDynamicSharedMemorySize, 73728);
  (void)hipFuncSetAttribute((const void*)k_fft1, hipFuncAttributeMaxDynamicSharedMemorySize, 73728);
  (void)hipFuncSetAttribute((const void*)k_attn<0>, hipFuncAttributeMaxDynamicSharedMemorySize, 133120);
  (void)hipFuncSetAttribute((const void*)k_attn<1>, hipFuncAttributeMaxDynamicSharedMemorySize, 133120);
  (void)hipFuncSetAttribute((const void*)k_out<2>, hipFuncAttributeMaxDynamicSharedMemorySize, 110592);
  (void)hipFuncSetAttribute((const void*)k_out<3>, hipFuncAttributeMaxDynamicSharedMemorySize, 110592);
  k_pre<<<310, 768, 73728, stream>>>(x0, wo, wr, wi, ws);
  k_fft1<<<279, 512, 73728, stream>>>(wqkv, ws);
  k_attn<0><<<256, 512, 133120, stream>>>(ws + O_XH1, ws);
  k_out<2><<<192, 512, 110592, stream>>>(ws, (char*)d_out);
  k_fft2<<<252, 512, 40960, stream>>>(wo, ws);
  k_attn<1><<<256, 512, 133120, stream>>>(ws + O_XH3, ws);
  k_out<3><<<192, 512, 110592, stream>>>(ws, (char*)d_out);
}
```

```cpp
#include <hip/hip_runtime.h>
#include <hip/hip_bf16.h>

typedef __bf16 bf16x8 __attribute__((ext_vector_type(8)));
typedef __bf16 bf16x2 __attribute__((ext_vector_type(2)));
typedef short s16x4 __attribute__((ext_vector_type(4)));
typedef float f32x4 __attribute__((ext_vector_type(4)));
typedef float f32x2 __attribute__((ext_vector_type(2)));
typedef unsigned u32x2 __attribute__((ext_vector_type(2)));
typedef unsigned u32x4 __attribute__((ext_vector_type(4)));
typedef unsigned short u16;
#define LDSP __attribute__((address_space(3)))
#define GLBP __attribute__((address_space(1)))
#define DI __device__ __forceinline__
#define FENCE() asm volatile("" ::: "memory")
#define WAITV(n) asm volatile("s_waitcnt vmcnt(" #n ")" ::: "memory")
#define SB() __builtin_amdgcn_sched_barrier(0)
#define BAR() do { SB(); asm volatile("s_waitcnt lgkmcnt(0)" ::: "memory"); __builtin_amdgcn_s_barrier(); FENCE(); SB(); } while (0)

#define STAMP(k, i) do {} while (0)
constexpr int S = 2048, D = 576;
constexpr size_t XB = (size_t)S * D * 2;
constexpr size_t O_XH0 = 0, O_XH1 = XB, O_XH2 = 2 * XB, O_XH3 = 3 * XB, O_OB = 4 * XB, O_X3 = 5 * XB, O_WT = 6 * XB;
constexpr size_t O_QF = 7 * XB, O_WOQ = O_QF + 663552, O_WOF = O_WOQ + 663552, O_WF = O_WOF + 663552;
constexpr size_t O_QTF = O_WF + 2064384, O_SS0 = O_QTF + 884736, O_SS2 = O_SS0 + 8192, O_G = O_SS2 + 6 * 8192;
constexpr int GP = 3072;
constexpr size_t GL = (size_t)256 * GP;

DI f32x4 mfma(bf16x8 a, bf16x8 b, f32x4 c) { return __builtin_amdgcn_mfma_f32_16x16x32_bf16(a, b, c, 0, 0, 0); }
DI unsigned pk2(float a, float b) { f32x2 f = {a, b}; return __builtin_bit_cast(unsigned, __builtin_convertvector(f, bf16x2)); }
DI bf16x8 pk8(f32x4 a, f32x4 b) { u32x4 u = {pk2(a[0], a[1]), pk2(a[2], a[3]), pk2(b[0], b[1]), pk2(b[2], b[3])}; return __builtin_bit_cast(bf16x8, u); }
DI float lo16(unsigned u) { return __builtin_bit_cast(float, u << 16); }
DI float hi16(unsigned u) { return __builtin_bit_cast(float, u & 0xffff0000u); }
DI float cosr(float rev) { return __builtin_amdgcn_cosf(__builtin_amdgcn_fractf(rev)); }
DI float sinr(float rev) { return __builtin_amdgcn_sinf(__builtin_amdgcn_fractf(rev)); }
DI void glds(const char* g, char* l) { __builtin_amdgcn_global_load_lds((const GLBP void*)g, (LDSP void*)l, 16, 0, 0); }
DI bf16x8 ldf(const char* p) { return *(const bf16x8*)p; }
typedef __amdgpu_buffer_rsrc_t rsrc_t;
DI rsrc_t mkrs(const void* p, unsigned bytes) { return __builtin_amdgcn_make_buffer_rsrc((void*)p, 0, bytes, 0x00020000); }
DI void bglds(rsrc_t r, char* l, int voff, int soff) { __builtin_amdgcn_raw_ptr_buffer_load_lds(r, (LDSP void*)l, 16, voff, soff, 0, 0); }
#define ALD16(dst, voff, sbase, imm) asm volatile("global_load_dwordx4 %0, %1, %2 offset:" #imm : "=v"(dst) : "v"(voff), "s"(sbase))
#define ALD8(dst, voff, sbase) asm volatile("global_load_dwordx2 %0, %1, %2" : "=v"(dst) : "v"(voff), "s"(sbase))
#define PIN(x) asm volatile("" : "+v"(x))
DI bf16x8 trf(const char* p, int d) {
  s16x4 lo = __builtin_amdgcn_ds_read_tr16_b64_v4i16((LDSP s16x4*)p), hi = __builtin_amdgcn_ds_read_tr16_b64_v4i16((LDSP s16x4*)(p + d));
  return __builtin_bit_cast(bf16x8, __builtin_shufflevector(lo, hi, 0, 1, 2, 3, 4, 5, 6, 7));
}
DI u32x2 pk4(f32x4 v) { return (u32x2){pk2(v[0], v[1]), pk2(v[2], v[3])}; }
constexpr u32x4 ONES = {0x3f803f80u, 0x3f803f80u, 0x3f803f80u, 0x3f803f80u};
constexpr f32x4 Z4 = {0.f, 0.f, 0.f, 0.f};
DI float qent(int d, int j) {
  int f = (j >> 1) + 1; float ph = (j & 1) ? 0.25f : 0.f, sc = 0.05892556509887896f;
  if (j >= 574) { sc = 0.041666666666666664f; ph = 0.f; f = (j == 575) ? 0 : 288; }
  return sc * cosr((float)(f * d) * (1.f / 576.f) + ph);
}
DI int operm(int k) {
  int hp = k / 36, c = k - 36 * hp, p = c >> 2, i = c & 3;
  return p < 8 ? 18 * (2 * hp + (p >> 2)) + 4 * (p & 3) + i : 18 * (2 * hp + (i >> 1)) + 16 + (i & 1);
}

DI void qgen(bf16x8* qf, int j, int d0, int dd) {
  int f = (j >> 1) + 1; float ph = (j & 1) ? 0.25f : 0.f, sc = 0.05892556509887896f;
  if (j >= 574) { sc = 0.041666666666666664f; ph = 0.f; f = (j == 575) ? 0 : 288; }
  const float st = (float)f * (1.f / 576.f), c2 = 2.f * __builtin_amdgcn_cosf(st), p0 = st * (float)d0 + ph;
#pragma unroll
  for (int i = 0; i < 9; ++i) {
    float p = __builtin_amdgcn_fractf(st * (float)(dd * i) + p0), v[8];
    v[0] = sc * __builtin_amdgcn_cosf(p); v[1] = sc * __builtin_amdgcn_cosf(p + st);
#pragma unroll
    for (int e = 2; e < 8; ++e) v[e] = c2 * v[e - 1] - v[e - 2];
    qf[i] = __builtin_bit_cast(bf16x8, (u32x4){pk2(v[0], v[1]), pk2(v[2], v[3]), pk2(v[4], v[5]), pk2(v[6], v[7])});
    PIN(qf[i]);
  }
}
template <int MODE>
DI void gemm_q(char* sm, int rb, int ng, const char* xs, char* out, float* ssq) {
  const int tid = threadIdx.x, w = __builtin_amdgcn_readfirstlane(tid >> 6), l = tid & 63, g = l >> 4, m = l & 15;
  const int kh = w >= 6, nt = w - 6 * kh, jt = 6 * ng + nt;
  STAMP(0, 0);
  {
    const rsrc_t xrs = mkrs(xs, MODE == 0 ? (unsigned)XB : 663552u);
    int voff[2];
#pragma unroll
    for (int h = 0; h < 2; ++h) {
      int r = 64 * rb + 8 * ((w + 4 * h) & 7) + (l >> 3);
      voff[h] = (MODE == 1 ? operm(r) : r) * 1152 + (((l & 7) ^ (l >> 3)) << 4);
    }
#pragma unroll
    for (int i = 0; i < 6; ++i) { int q = w + 12 * i; bglds(xrs, sm + q * 1024, voff[i & 1], (q >> 3) * 128); }
  }
  SB(); STAMP(0, 1);
  bf16x8 qf[9];
  qgen(qf, 16 * jt + m, 8 * g + 32 * kh, 64);
  WAITV(0); BAR(); STAMP(0, 2);
  f32x4 acc[4] = {}, gr = Z4, su = Z4;
  const bool stats = MODE == 0 && ng == 0 && nt < 4;
  const int x2 = 2 * kh;
  const char* xb = sm + (m >> 3) * 1024 + (m & 7) * 128 + (((4 * kh + g) ^ (m & 7)) << 4);
#pragma unroll
  for (int i = 0; i < 9; ++i) {
#pragma unroll
    for (int a = 0; a < 4; ++a) {
      bf16x8 xf = ldf(xb + i * 8192 + (a ^ x2) * 2048);
      acc[a] = MODE == 1 ? mfma(xf, qf[i], acc[a]) : mfma(qf[i], xf, acc[a]);
    }
    if (stats) { bf16x8 xf = ldf(xb + i * 8192 + nt * 2048); gr = mfma(xf, xf, gr); su = mfma(__builtin_bit_cast(bf16x8, ONES), xf, su); }
  }
  BAR(); STAMP(0, 4);
  *(f32x4*)(sm + w * 4096 + l * 16) = acc[2]; *(f32x4*)(sm + w * 4096 + 1024 + l * 16) = acc[3];
  if (stats && kh) { *(f32x4*)(sm + w * 4096 + 2048 + l * 16) = gr; *(f32x4*)(sm + w * 4096 + 3072 + l * 16) = su; }
  BAR();
  const char* pp = sm + (kh ? w - 6 : w + 6) * 4096 + l * 16;
#pragma unroll
  for (int a = 0; a < 2; ++a) {
    f32x4 v = acc[a] + *(const f32x4*)(pp + a * 1024);
    const int rt = 4 * rb + x2 + a, n = 16 * jt + 4 * g;
    if (MODE == 1) *(u32x2*)(out + (jt * 18 + (rt >> 1)) * 1024 + (m + 16 * (2 * (rt & 1) + (g >> 1))) * 16 + (g & 1) * 8) = pk4(v);
    else *(u32x2*)(out + ((n >> 3) * S + 16 * rt + m) * 16 + (n & 4) * 2) = pk4(v);
  }
  if (stats && !kh) {
    gr += *(const f32x4*)(pp + 2048); su += *(const f32x4*)(pp + 3072);
    float dsel = (m & 2) ? ((m & 1) ? gr[3] : gr[2]) : ((m & 1) ? gr[1] : gr[0]);
    if ((m >> 2) == g) ssq[64 * rb + 16 * nt + m] = fmaxf(dsel - su[0] * su[0] * (1.f / 576.f), 0.f);
  }
  FENCE(); STAMP(0, 5);
}

template <int NP>
DI void fft_cols(char* sm, int b, const char* xin, char* xout, const float* ssq, const char* wt) {
  const int tid = threadIdx.x, w8 = __builtin_amdgcn_readfirstlane(tid >> 6), l = tid & 63, g = l >> 4, m = l & 15;
  const int w = w8 & 3, hi = w8 >> 2;
  const int kb = (b & 7) + 8 * (b >> 4), half = (b >> 3) & 1;
  const bool last = kb == 71 && half == 1;
  constexpr int KID = NP == 1 ? 1 : 4;
  STAMP(KID, 0);
  f32x4 ps[NP];
#pragma unroll
  for (int p = 0; p < NP; ++p) ps[p] = *(const f32x4*)(ssq + p * S + tid * 4);
  u32x2 xv[4];
  const int xo = (kb * S + 32 * (16 * w + 4 * g) + 16 * hi + m) * 16 + 8 * half;
#pragma unroll
  for (int r = 0; r < 4; ++r) xv[r] = *(const u32x2*)(xin + xo + r * 512);
  u32x4 wv[2];
#pragma unroll
  for (int t = 0; t < 2; ++t) wv[t] = *(const u32x4*)(wt + (((4 * kb + 2 * half + hi) * 4 + w) * 2 + t) * 1024 + l * 16);
  FENCE(); STAMP(KID, 1);
  SB();
  bf16x8 c64[2], s64[2], n64[2], c32[2], s32[2], n32[2];
  f32x4 t1c[2], t1s[2], t2c[2], t2s[2];
  {
    const float k1 = (float)(16 * w + m), g4 = (float)(4 * g);
    const unsigned sg = (m & 1) ? 0x80008000u : 0u;
    float c[8], sn[8];
    const float a = k1 * (1.f / 64.f), a2 = 2.f * __builtin_amdgcn_cosf(a), p = a * (g4 + g4);
    c[0] = __builtin_amdgcn_cosf(p); c[1] = __builtin_amdgcn_cosf(p + a); sn[0] = __builtin_amdgcn_sinf(p); sn[1] = __builtin_amdgcn_sinf(p + a);
#pragma unroll
    for (int j = 2; j < 8; ++j) { c[j] = a2 * c[j - 1] - c[j - 2]; sn[j] = a2 * sn[j - 1] - sn[j - 2]; }
    u32x4 uc = {pk2(c[0], c[1]), pk2(c[2], c[3]), pk2(c[4], c[5]), pk2(c[6], c[7])}, us = {pk2(sn[0], sn[1]), pk2(sn[2], sn[3]), pk2(sn[4], sn[5]), pk2(sn[6], sn[7])};
    c64[0] = __builtin_bit_cast(bf16x8, uc); c64[1] = __builtin_bit_cast(bf16x8, uc ^ sg);
    s64[0] = __builtin_bit_cast(bf16x8, us); s64[1] = __builtin_bit_cast(bf16x8, us ^ sg);
    n64[0] = __builtin_bit_cast(bf16x8, us ^ 0x80008000u); n64[1] = __builtin_bit_cast(bf16x8, us ^ (sg ^ 0x80008000u));
    const float e = (float)m * (1.f / 32.f);
#pragma unroll
    for (int j = 0; j < 4; ++j) { float q = e * (g4 + (float)j); c[j] = __builtin_amdgcn_cosf(q); sn[j] = __builtin_amdgcn_sinf(q); }
    unsigned c0 = pk2(c[0], c[1]), c1 = pk2(c[2], c[3]), s0 = pk2(sn[0], sn[1]), s1 = pk2(sn[2], sn[3]);
    uc = (u32x4){c0, c1, c0 ^ sg, c1 ^ sg}; us = (u32x4){s0, s1, s0 ^ sg, s1 ^ sg};
    c32[0] = __builtin_bit_cast(bf16x8, uc); c32[1] = __builtin_bit_cast(bf16x8, uc ^ 0x80000000u);
    s32[0] = __builtin_bit_cast(bf16x8, us); s32[1] = __builtin_bit_cast(bf16x8, us ^ 0x80000000u);
    n32[0] = __builtin_bit_cast(bf16x8, us ^ 0x80008000u); n32[1] = __builtin_bit_cast(bf16x8, us ^ 0x00008000u);
    const float b1 = k1 * (1.f / 2048.f), b12 = 2.f * __builtin_amdgcn_cosf(b1);
#pragma unroll
    for (int h = 0; h < 2; ++h) {
      const float p1 = b1 * (g4 + (float)(16 * h)), b2 = (float)(16 * h + m) * (1.f / 2048.f), b22 = 2.f * __builtin_amdgcn_cosf(b2), p2 = b2 * (g4 + (float)(16 * w));
      t1c[h][0] = __builtin_amdgcn_cosf(p1); t1c[h][1] = __builtin_amdgcn_cosf(p1 + b1); t1s[h][0] = __builtin_amdgcn_sinf(p1); t1s[h][1] = __builtin_amdgcn_sinf(p1 + b1);
      t2c[h][0] = __builtin_amdgcn_cosf(p2); t2c[h][1] = __builtin_amdgcn_cosf(p2 + b2); t2s[h][0] = __builtin_amdgcn_sinf(p2); t2s[h][1] = __builtin_amdgcn_sinf(p2 + b2);
#pragma unroll
      for (int r = 2; r < 4; ++r) {
        t1c[h][r] = b12 * t1c[h][r - 1] - t1c[h][r - 2]; t1s[h][r] = b12 * t1s[h][r - 1] - t1s[h][r - 2];
        t2c[h][r] = b22 * t2c[h][r - 1] - t2c[h][r - 2]; t2s[h][r] = b22 * t2s[h][r - 1] - t2s[h][r - 2];
      }
      PIN(c64[h]); PIN(s64[h]); PIN(n64[h]); PIN(c32[h]); PIN(s32[h]); PIN(n32[h]); PIN(t1c[h]); PIN(t1s[h]); PIN(t2c[h]); PIN(t2s[h]);
    }
  }
  SB();
  FENCE(); STAMP(KID, 2);
  float* rsl = (float*)(sm + 32768);
  {
    f32x4 a = ps[0];
#pragma unroll
    for (int p = 1; p < NP; ++p) a += ps[p];
#pragma unroll
    for (int i = 0; i < 4; ++i) a[i] = __builtin_amdgcn_rsqf(a[i] * (1.f / 576.f) + 1e-5f) * (1.f / 2048.f);
    *(f32x4*)(rsl + tid * 4) = a;
  }
  BAR();
  const int wo = m * 128 + (((2 * w + (g >> 1)) ^ (m & 7)) << 4) + (g & 1) * 8;
  {
    f32x4 u[4];
#pragma unroll
    for (int r = 0; r < 4; ++r) {
      float rs = rsl[32 * (16 * w + 4 * g + r) + 16 * hi + m];
      u[0][r] = lo16(xv[r][0]) * rs; u[1][r] = hi16(xv[r][0]) * rs; u[2][r] = lo16(xv[r][1]) * rs;
      u[3][r] = last ? 0.f : hi16(xv[r][1]) * rs;
    }
#pragma unroll
    for (int p = 0; p < 4; ++p) *(u32x2*)(sm + p * 4096 + hi * 2048 + wo) = pk4(u[p]);
  }
  BAR(); STAMP(KID, 3);
  const int ro0 = m * 128 + ((g ^ (m & 7)) << 4), ro1 = ro0 ^ 64;
  {
    f32x4 re[2], im[2];
#pragma unroll
    for (int t = 0; t < 2; ++t) {
      const char* p = sm + hi * 8192 + t * 2048;
      bf16x8 a0 = ldf(p + ro0), a1 = ldf(p + ro1), b0 = ldf(p + 4096 + ro0), b1 = ldf(p + 4096 + ro1);
      f32x4 x = mfma(a0, c64[0], Z4), y = mfma(b0, c64[0], Z4);
      x = mfma(a1, c64[1], x); y = mfma(b1, c64[1], y);
      x = mfma(b0, s64[0], x); y = mfma(a0, n64[0], y);
      x = mfma(b1, s64[1], x); y = mfma(a1, n64[1], y);
#pragma unroll
      for (int r = 0; r < 4; ++r) { re[t][r] = x[r] * t1c[t][r] + y[r] * t1s[t][r]; im[t][r] = y[r] * t1c[t][r] - x[r] * t1s[t][r]; }
    }
    bf16x8 fr = pk8(re[0], re[1]), fi = pk8(im[0], im[1]);
#pragma unroll
    for (int t = 0; t < 2; ++t) {
      f32x4 x = mfma(c32[t], fr, Z4), y = mfma(c32[t], fi, Z4);
      x = mfma(s32[t], fi, x); y = mfma(n32[t], fr, y);
#pragma unroll
      for (int r = 0; r < 4; ++r) {
        float a = lo16(wv[t][r]), bb = hi16(wv[t][r]);
        re[t][r] = x[r] * a - y[r] * bb; im[t][r] = x[r] * bb + y[r] * a;
      }
    }
    fr = pk8(re[0], re[1]); fi = pk8(im[0], im[1]);
#pragma unroll
    for (int t = 0; t < 2; ++t) {
      f32x4 x = mfma(fr, c32[t], Z4), y = mfma(fi, c32[t], Z4);
      x = mfma(fi, n32[t], x); y = mfma(fr, s32[t], y);
      f32x4 vr, vi;
#pragma unroll
      for (int r = 0; r < 4; ++r) { vr[r] = x[r] * t2c[t][r] - y[r] * t2s[t][r]; vi[r] = y[r] * t2c[t][r] + x[r] * t2s[t][r]; }
      char* p = sm + 16384 + hi * 8192 + t * 2048 + wo;
      *(u32x2*)p = pk4(vr); *(u32x2*)(p + 4096) = pk4(vi);
    }
  }
  BAR(); STAMP(KID, 4);
  {
    f32x4 yr[2], yi[2];
#pragma unroll
    for (int c = 0; c < 2; ++c) {
      const char* p = sm + 16384 + c * 8192 + hi * 2048;
      bf16x8 a0 = ldf(p + ro0), a1 = ldf(p + ro1), b0 = ldf(p + 4096 + ro0), b1 = ldf(p + 4096 + ro1);
      f32x4 x = mfma(c64[0], a0, Z4), y = mfma(c64[0], b0, Z4);
      x = mfma(c64[1], a1, x); y = mfma(c64[1], b1, y);
      x = mfma(n64[0], b0, x); y = mfma(s64[0], a0, y);
      x = mfma(n64[1], b1, x); y = mfma(s64[1], a1, y);
      yr[c] = x; yi[c] = y;
    }
#pragma unroll
    for (int r = 0; r < 4; ++r) {
      u32x2 o = {pk2(lo16(xv[r][0]) + yr[0][r], hi16(xv[r][0]) + yi[0][r]),
                 pk2(lo16(xv[r][1]) + yr[1][r], hi16(xv[r][1]) + (last ? 0.f : yi[1][r]))};
      *(u32x2*)(xout + xo + r * 512) = o;
    }
  }
  FENCE(); STAMP(KID, 5);
}

DI void wqkv_rot(char* sm, int u, const char* wqkv, char* wf) {
  const int tid = threadIdx.x, w = __builtin_amdgcn_readfirstlane(tid >> 6), l = tid & 63, g = l >> 4, m = l & 15;
  const int jg = u & 3, ng = u >> 2, kh = w >> 2, wc = w & 3, jt = 9 * jg + 2 * wc + kh, j8 = 9 * jg + 8;
  STAMP(1, 0);
  const rsrc_t rs = mkrs(wqkv, 1990656u);
  const int voff = (l >> 3) * 3456 + (((((l >> 1) & 3) ^ (((l >> 4) & 1) | ((w & 1) << 1))) << 5) | ((l & 1) << 4));
  const int l16 = l * 16;
#pragma unroll
  for (int i = 0; i < 9; ++i) bglds(rs, sm + (w + 8 * i) * 1024, voff, (w + 8 * i) * 27648 + ng * 128);
  SB();
  bf16x8 af[3][9];
  qgen(af[0], 16 * jt + m, 288 * kh + 8 * g, 32);
  qgen(af[1], 16 * (jt + 1 - 2 * kh) + m, 288 * kh + 8 * g, 32);
  qgen(af[2], 16 * j8 + m, 288 * kh + 8 * g, 32);
  int ob[4];
#pragma unroll
  for (int t = 0; t < 4; ++t) {
    int n = 64 * ng + 16 * (t ^ wc) + m, seg = (n >= 576) + (n >= 1152), c = n - 576 * seg, hp = c / 36;
    int cc = c - 36 * hp + (seg == 0 ? 72 : seg == 1 ? 0 : 36);
    ob[t] = (7 * hp + (cc >> 4)) * 18432 + (cc & 15) * 16;
  }
  STAMP(1, 2);
  WAITV(0); BAR(); STAMP(1, 3);
  f32x4 acc[2][4] = {}, a8 = Z4;
  const int rbase = 9 * kh * 4096 + (8 * g + (m >> 2)) * 128 + 8 * (m & 3), fx = (((m >> 3) & 1) | ((g & 1) << 1)) ^ wc;
#pragma unroll
  for (int i = 0; i < 9; ++i) {
#pragma unroll
    for (int t = 0; t < 4; ++t) {
      bf16x8 bfr = trf(sm + i * 4096 + rbase + ((t ^ fx) << 5), 512);
      acc[0][t] = mfma(af[0][i], bfr, acc[0][t]); acc[1][t] = mfma(af[1][i], bfr, acc[1][t]);
      if (t == 0) a8 = mfma(af[2][i], bfr, a8);
    }
  }
  BAR(); STAMP(1, 4);
#pragma unroll
  for (int t = 0; t < 4; ++t) *(f32x4*)(sm + (w * 5 + t) * 1024 + l16) = acc[1][t];
  *(f32x4*)(sm + (w * 5 + 4) * 1024 + l16) = a8;
  BAR();
  const int jo = (jt >> 1) * 1024 + (2 * (jt & 1) + (g >> 1)) * 256 + (g & 1) * 8;
#pragma unroll
  for (int t = 0; t < 4; ++t) *(u32x2*)(wf + ob[t] + jo) = pk4(acc[0][t] + *(const f32x4*)(sm + ((w ^ 4) * 5 + t) * 1024 + l16));
  if (!kh) {
    f32x4 o = a8 + *(const f32x4*)(sm + ((w ^ 4) * 5 + 4) * 1024 + l16);
    if (j8 == 35 && g == 3) o[3] = 0.f;
    *(u32x2*)(wf + ob[0] + (j8 >> 1) * 1024 + (2 * (j8 & 1) + (g >> 1)) * 256 + (g & 1) * 8) = pk4(o);
  }
  FENCE(); STAMP(1, 5);
}

extern "C" __global__ void __launch_bounds__(512, 2) k_fft1(const char* wqkv, char* ws) {
  extern __shared__ __attribute__((aligned(16))) char sm[];
  const int b = blockIdx.x;
  if (b < 144) fft_cols<1>(sm, b, ws + O_XH0, ws + O_XH1, (const float*)(ws + O_SS0), ws + O_WT);
  else wqkv_rot(sm, b - 144, wqkv, ws + O_WF);
}
__global__ void __launch_bounds__(512, 2) k_fft2(const char* wo, char* ws) {
  extern __shared__ __attribute__((aligned(16))) char sm[];
  const int b = blockIdx.x;
  if (b < 144) fft_cols<6>(sm, b, ws + O_XH2, ws + O_XH3, (const float*)(ws + O_SS2), ws + O_WT);
  else {
    const int e = (b - 144) * 512 + threadIdx.x, l = e & 63, pc = e >> 6, nt = pc / 18, ks = pc - 18 * nt;
    unsigned h[8];
    if (e < 41472) {
      const int r0 = operm(32 * ks + 8 * (l >> 4)), r1 = operm(32 * ks + 8 * (l >> 4) + 4), c0 = (32 * ks + 8 * (l >> 4)) % 36;
#pragma unroll
      for (int j = 0; j < 8; ++j) {
        int r = (j < 4 ? r0 : r1) + (c0 == (j < 4 ? 32 : 28) ? 18 * ((j >> 1) & 1) + (j & 1) : (j & 3));
        h[j] = *(const u16*)(wo + r * 1152 + (16 * nt + (l & 15)) * 2);
      }
    }
    if (e < (int)(GL / 16)) *(u32x4*)(ws + O_G + GL + e * 16) = (u32x4){0u, 0u, 0u, 0u};
    const int hp = nt / 3, c = 16 * (nt - 3 * hp) + (l & 15), d = 36 * hp + (c < 36 ? c : 0), j0 = 32 * ks + 8 * (l >> 4);
    *(u32x4*)(ws + O_QTF + e * 16) = (u32x4){pk2(qent(d, j0), qent(d, j0 + 1)), pk2(qent(d, j0 + 2), qent(d, j0 + 3)),
                                             pk2(qent(d, j0 + 4), qent(d, j0 + 5)), pk2(qent(d, j0 + 6), qent(d, j0 + 7))};
    if (e < 41472) *(u32x4*)(ws + O_WOF + e * 16) = (u32x4){h[0] | (h[1] << 16), h[2] | (h[3] << 16), h[4] | (h[5] << 16), h[6] | (h[7] << 16)};
  }
}

constexpr int A_KV = 129024;
constexpr int A_PK = 65536, A_V = A_PK + 13056, A_Q = A_V + 13056;
template <int LAST>
DI void attn_body(const char* xin, char* ws) {
  extern __shared__ __attribute__((aligned(16))) char sm[];
  const int tid = threadIdx.x, w = __builtin_amdgcn_readfirstlane(tid >> 6), l = tid & 63, g = l >> 4, m = l & 15;
  const int b = blockIdx.x, rb = 4 * ((b & 7) >> 1) + ((b >> 3) & 3), hp = 8 * (b & 1) + (b >> 5);
  const int wr = w & 3, kh = w >> 2, s0 = 128 * rb + 32 * wr;
  constexpr int KID = LAST ? 5 : 2;
  STAMP(KID, 0);
  const rsrc_t wrs = mkrs(ws + O_WF + hp * 129024, 129024);
  const int l16 = l * 16, xo0 = (g * S + s0 + 16 * kh + m) * 16, xo1 = xo0 ^ 256;
  bf16x8 xf[2][9];
  auto issue = [&](int j) {
#pragma unroll
    for (int i = 0; i < 2; ++i) {
      int q = w + 8 * i; q = q < 14 ? q : q - 8;
      int pc = (q >> 1) * 18 + 2 * j + (q & 1);
      bglds(wrs, sm + pc * 1024, l16, pc * 1024);
    }
    const char* xb = xin + (2 * j + kh) * (4 * S * 16);
    ALD16(xf[0][j], xo0, xb, 0); ALD16(xf[1][j], xo1, xb, 0);
    FENCE();
  };
#pragma unroll
  for (int j = 0; j < 3; ++j) issue(j);
  if (tid < 256) *(u32x4*)(sm + A_KV + tid * 16) = (u32x4){0u, 0u, 0u, 0u};
  const int pa = w < 6 ? w / 3 : 2, pb = w < 6 ? w - 3 * pa : w - 5;
  const int dq = 4 * pa + g, ec = 16 * pb + m - 4, eh = ec >= 18;
  const bool isz = ec == 36, valid = isz ? dq < 9 : (ec >= 0 && ec < 36 && dq >= 4 * eh && dq <= 4 * eh + 4);
  const int slot = (isz ? 180 + dq : 36 * (dq - 4 * eh) + ec) * 16;
  STAMP(KID, 1);
  f32x4 acc[2][8] = {};
  bf16x8 wf[2][7];
  WAITV(8); BAR(); STAMP(KID, 2);
#pragma unroll
  for (int t = 0; t < 7; ++t) wf[0][t] = ldf(sm + (t * 18 + kh) * 1024 + l16);
  issue(3);
#pragma unroll
  for (int i = 0; i < 9; ++i) {
    if (i < 8) {
      if (i <= 5) WAITV(8); else if (i == 6) WAITV(4); else WAITV(0);
      BAR();
#pragma unroll
      for (int t = 0; t < 7; ++t) wf[(i + 1) & 1][t] = ldf(sm + (t * 18 + 2 * i + 2 + kh) * 1024 + l16);
      if (i + 4 < 9) issue(i + 4);
      SB();
    }
    PIN(xf[0][i]); PIN(xf[1][i]);
#pragma unroll
    for (int a = 0; a < 2; ++a) {
      bf16x8 xm = xf[a][i];
      if (i == 8) { u32x4 u = __builtin_bit_cast(u32x4, xm); u[3] = (g == 3 && kh) ? (u[3] & 0xffffu) : u[3]; xm = __builtin_bit_cast(bf16x8, u); }
      acc[a][7] = mfma(xm, xm, acc[a][7]);
    }
#pragma unroll
    for (int t = 0; t < 7; ++t) { acc[0][t] = mfma(xf[0][i], wf[i & 1][t], acc[0][t]); acc[1][t] = mfma(xf[1][i], wf[i & 1][t], acc[1][t]); }
  }
  BAR(); STAMP(KID, 3);
#pragma unroll
  for (int t = 0; t < 8; ++t) *(f32x4*)(sm + (w * 8 + t) * 1024 + l16) = acc[1][t];
  BAR();
  f32x4 v[8];
#pragma unroll
  for (int t = 0; t < 8; ++t) v[t] = acc[0][t] + *(const f32x4*)(sm + ((w ^ 4) * 8 + t) * 1024 + l16);
  {
    f32x4 rs, rl;
#pragma unroll
    for (int r = 0; r < 4; ++r) { rs[r] = __builtin_amdgcn_rsqf(__shfl(v[7][r], 20 * g + r) * (1.f / 576.f) + 1e-5f); rl[r] = rs[r] * 1.4426950408889634f; }
    const int so = (32 * wr + 16 * kh + 4 * g) * 2 + m * 272;
#pragma unroll
    for (int t = 0; t < 7; ++t) {
      if (t != 3) {
        f32x4 p;
#pragma unroll
        for (int r = 0; r < 4; ++r) p[r] = fmaxf(v[t][r] * rs[r] + 1e-6f, 1e-6f) + __builtin_amdgcn_exp2f(fminf(v[t][r] * rl[r], 0.f));
        if (t < 3) *(u32x2*)(sm + A_PK + t * 4352 + so) = pk4(p);
        else *(u32x2*)(sm + A_Q + w * 1536 + (16 * (t - 4) + m) * 32 + g * 8) = pk4(p);
      }
      if (t >= 2 && t <= 4) {
        u32x2 u = pk4(v[t] * rs);
        if (t == 4 && m == 8) u = (u32x2){0x3f803f80u, 0x3f803f80u};
        *(u32x2*)(sm + A_V + (t - 2) * 4352 + so) = u;
      }
    }
  }
  BAR(); STAMP(KID, 4);
  const rsrc_t grs = mkrs(ws + O_G + (LAST ? GL : 0) + hp * 16 * GP, 16 * GP);
  if (LAST) {
    const rsrc_t qrs = mkrs(ws + O_QTF + hp * 55296, 55296);
#pragma unroll
    for (int i = 0; i < 7; ++i) { int q = w + 8 * i; q = q < 54 ? q : q - 8; bglds(qrs, sm + q * 1024, l16, q * 1024); }
  }
  {
    f32x4 c = Z4;
#pragma unroll
    for (int k = 0; k < 4; ++k)
      c = mfma(ldf(sm + A_PK + pa * 4352 + m * 272 + k * 64 + g * 16), ldf(sm + A_V + pb * 4352 + m * 272 + k * 64 + g * 16), c);
    u32x4 u = ((__builtin_bit_cast(u32x4, c) + 0x80u) & 0xffffff00u) | 0x5au;
    if (valid) __builtin_amdgcn_raw_buffer_store_b128(u, grs, slot + rb * GP, 0, 16);
  }
  FENCE(); STAMP(KID, 5);
  if (LAST) {
    WAITV(1); BAR();
    f32x4 y[2][3] = {};
#pragma unroll
    for (int i = 0; i < 9; ++i)
#pragma unroll
      for (int t = 0; t < 3; ++t) {
        bf16x8 qf = ldf(sm + (t * 18 + 2 * i + kh) * 1024 + l16);
        y[0][t] = mfma(qf, xf[0][i], y[0][t]); y[1][t] = mfma(qf, xf[1][i], y[1][t]);
      }
    char* ex = sm + A_PK;
#pragma unroll
    for (int t = 0; t < 3; ++t) *(f32x4*)(ex + (w * 3 + t) * 1024 + l16) = y[1][t];
    BAR();
#pragma unroll
    for (int t = 0; t < 3; ++t) {
      f32x4 o = y[0][t] + *(const f32x4*)(ex + ((w ^ 4) * 3 + t) * 1024 + l16);
      if (t < 2 || g == 0) *(u32x2*)(ws + O_X3 + (s0 + 16 * kh + m) * 1152 + (36 * hp + 16 * t + 4 * g) * 2) = pk4(o);
    }
  }
  FENCE(); STAMP(KID, 6);
  {
    u32x4 r[16];
    for (int spin = 0; spin < 100000; ++spin) {
      FENCE();
      unsigned ok = 0xffu;
      if (valid) {
#pragma unroll
        for (int p = 0; p < 16; ++p) r[p] = __builtin_amdgcn_raw_buffer_load_b128(grs, slot, p * GP, 16);
#pragma unroll
        for (int p = 0; p < 16; ++p) ok &= (r[p][0] & r[p][1]) & (r[p][2] & r[p][3]);
      } else ok = 0x5au;
      if (__all(ok == 0x5au)) break;
      __builtin_amdgcn_s_sleep(1);
    }
    if (valid) {
      f32x4 sum = Z4;
#pragma unroll
      for (int p = 0; p < 16; ++p) sum += __builtin_bit_cast(f32x4, r[p] & 0xffffff00u);
      u32x2 u = pk4(sum);
      if (isz ? dq <= 4 : !eh) *(u32x2*)(sm + A_KV + (isz ? 18 : ec) * 64 + 16 + 8 * dq) = (u32x2){u[0], dq == 4 ? 0u : u[1]};
      if (isz ? dq >= 4 : eh) *(u32x2*)(sm + A_KV + (isz ? 50 : ec + 14) * 64 - 16 + 8 * dq) = (u32x2){dq == 4 ? 0u : u[0], u[1]};
    }
  }
  BAR(); STAMP(KID, 7);
  {
    unsigned tail[2];
    char* ob = ws + O_OB + (s0 + 16 * kh + m) * 16;
#pragma unroll
    for (int h = 0; h < 2; ++h) {
      bf16x8 k0 = ldf(sm + A_KV + (32 * h + m) * 64 + g * 16), k1 = ldf(sm + A_KV + (32 * h + 16 + m) * 64 + g * 16);
      bf16x8 pf = trf(sm + A_Q + w * 1536 + (16 * h + 8 * g + (m >> 2)) * 32 + (m & 3) * 8, 128);
      f32x4 n0 = mfma(k0, pf, Z4), n1 = mfma(k1, pf, Z4);
      float inv = __builtin_amdgcn_rcpf(__shfl(n1[2], m) + 1e-6f);
      int P = 9 * hp + 4 * h + g;
      *(u32x2*)(ob + (P >> 1) * (S * 16) + (P & 1) * 8) = pk4(n0 * inv);
      tail[h] = pk2(n1[0] * inv, n1[1] * inv);
    }
    int P = 9 * hp + 8;
    if (g == 0) *(u32x2*)(ob + (P >> 1) * (S * 16) + (P & 1) * 8) = (u32x2){tail[0], tail[1]};
  }
  FENCE(); STAMP(KID, 8);
}

template <int MODE>
DI void out_body(char* ws, char* outp) {
  extern __shared__ __attribute__((aligned(16))) char sm[];
  const int tid = threadIdx.x, w = __builtin_amdgcn_readfirstlane(tid >> 6), l = tid & 63, g = l >> 4, m = l & 15;
  const int b = blockIdx.x, rb = 8 * ((b & 7) >> 1) + ((b >> 3) & 7), ng = 3 * (b & 1) + (b >> 6);
  const int wr = w & 1, ch = (w >> 1) & 1, kh = w >> 2, s = 64 * rb + 32 * wr + 16 * kh + m;
  constexpr int KID = MODE == 2 ? 3 : 6;
  STAMP(KID, 0);
  const rsrc_t wrs = mkrs(ws + (MODE == 2 ? O_WOQ : O_WOF) + ng * 110592, 110592);
  const int l16 = l * 16, xo0 = (g * S + s) * 16, xo1 = xo0 ^ 256;
  bf16x8 xf[2][9];
  auto issue = [&](int st) {
#pragma unroll
    for (int i = 0; i < (st < 4 ? 3 : 2); ++i) {
      int q = w + 8 * i; if (st == 4) q = q < 12 ? q : q - 8;
      int pc = st < 4 ? (q >> 2) * 18 + 4 * st + (q & 3) : (q >> 1) * 18 + 16 + (q & 1);
      bglds(wrs, sm + pc * 1024, l16, pc * 1024);
    }
#pragma unroll
    for (int i = 2 * st; i < (st < 4 ? 2 * st + 2 : 9); ++i) {
      const char* xb = ws + O_OB + (2 * i + kh) * (4 * S * 16);
      ALD16(xf[0][i], xo0, xb, 0); ALD16(xf[1][i], xo1, xb, 0);
    }
    FENCE();
  };
  issue(0); issue(1);
  u32x2 rv[3];
  int ro[3];
#pragma unroll
  for (int t = 0; t < 3; ++t) {
    int n = 16 * (6 * ng + 3 * ch + t) + 4 * g;
    ro[t] = MODE == 2 ? ((n >> 3) * S + s) * 16 + (n & 4) * 2 : s * 1152 + n * 2;
  }
  STAMP(KID, 1);
  f32x4 acc[6] = {};
  bf16x8 wf[2][6];
  const char* wa = sm + (54 * ch + kh) * 1024 + l16;
  WAITV(7); BAR(); STAMP(KID, 2);
#pragma unroll
  for (int t = 0; t < 6; ++t) wf[0][t] = ldf(wa + ((t % 3) * 18 + 2 * (t / 3)) * 1024);
  issue(2);
#pragma unroll
  for (int st = 0; st < 5; ++st) {
    if (st < 4) {
      if (st < 3) WAITV(7); else WAITV(3);
      BAR();
#pragma unroll
      for (int t = 0; t < (st < 3 ? 6 : 3); ++t) wf[(st + 1) & 1][t] = ldf(wa + ((t % 3) * 18 + 4 * st + 4 + 2 * (t / 3)) * 1024);
      if (st < 2) issue(st + 3);
      if (st == 1) {
        const char* rp = ws + (MODE == 2 ? O_XH1 : O_X3);
        ALD8(rv[0], ro[0], rp); ALD8(rv[1], ro[1], rp); ALD8(rv[2], ro[2], rp);
      }
      SB();
    }
#pragma unroll
    for (int k = 0; k < (st < 4 ? 2 : 1); ++k) {
      PIN(xf[0][2 * st + k]); PIN(xf[1][2 * st + k]);
#pragma unroll
      for (int t = 0; t < 3; ++t) {
        acc[t] = mfma(wf[st & 1][3 * k + t], xf[0][2 * st + k], acc[t]); acc[3 + t] = mfma(wf[st & 1][3 * k + t], xf[1][2 * st + k], acc[3 + t]);
      }
    }
  }
  BAR(); STAMP(KID, 4);
#pragma unroll
  for (int t = 0; t < 3; ++t) *(f32x4*)(sm + (w * 3 + t) * 1024 + l16) = acc[3 + t];
  WAITV(0); BAR();
  char* op = MODE == 2 ? ws + O_XH2 : outp;
  float sq = 0.f;
#pragma unroll
  for (int t = 0; t < 3; ++t) {
    PIN(rv[t]);
    f32x4 v = acc[t] + *(const f32x4*)(sm + ((w ^ 4) * 3 + t) * 1024 + l16);
    v[0] += lo16(rv[t][0]); v[1] += hi16(rv[t][0]); v[2] += lo16(rv[t][1]); v[3] += hi16(rv[t][1]);
    *(u32x2*)(op + ro[t]) = pk4(v);
    if (MODE == 2) sq += v[0] * v[0] + v[1] * v[1] + v[2] * v[2] + ((t == 2 && ng == 5 && ch && g == 3) ? 0.f : v[3] * v[3]);
  }
  if (MODE == 2) {
    sq += __shfl_xor(sq, 16); sq += __shfl_xor(sq, 32);
    float* red = (float*)(sm + 32768);
    if (ch && g == 0) red[(2 * wr + kh) * 16 + m] = sq;
    BAR();
    if (!ch && g == 0) ((float*)(ws + O_SS2))[ng * S + s] = sq + red[(2 * wr + kh) * 16 + m];
  }
  FENCE(); STAMP(KID, 5);
}

__global__ void __launch_bounds__(512, 2) k_attn0(char* ws) { attn_body<0>(ws + O_XH1, ws); }
__global__ void __launch_bounds__(512, 2) k_out2(char* ws) { out_body<2>(ws, nullptr); }
__global__ void __launch_bounds__(512, 2) k_attn1(char* ws) { attn_body<1>(ws + O_XH3, ws); }
__global__ void __launch_bounds__(512, 2) k_out3(char* ws, char* outp) { out_body<3>(ws, outp); }

__global__ void __launch_bounds__(768, 6) k_pre(const char* x0, const char* wo, const char* wr, const char* wi, char* ws) {
  extern __shared__ __attribute__((aligned(16))) char sm[];
  const int b = blockIdx.x, tid = threadIdx.x;
  if (b < 192) {
    int x = b & 7, i = b >> 3;
    gemm_q<0>(sm, 4 * x + (i & 3), i >> 2, x0, ws + O_XH0, (float*)(ws + O_SS0));
  } else if (b < 246) {
    int u = b - 192;
    gemm_q<1>(sm, u % 9, u / 9, wo, ws + O_WOQ, nullptr);
  } else {
    STAMP(0, 0);
    int u = b - 246, tg = u >> 3, kb = 8 * (u & 7);
    u32x4 v[4], code;
    {
      const char* pc; int len;
      asm volatile("s_getpc_b64 %0\n.Lpre_pc:\n\ts_mov_b32 %1, .Lpre_pc-k_fft1" : "=s"(pc), "=s"(len));
      int off = tg * 12288 + tid * 16;
      off = off + 16 <= len ? off : 0;
      const char* base = len > 0 ? pc - len : pc;
      ALD16(code, off, base, 0);
    }
#pragma unroll
    for (int i = 0; i < 4; ++i) {
      int e = i * 768 + tid; e = e < 2312 ? e : 2311;
      int run = e / 289, o = e - 289 * run;
      v[i] = *(const u32x4*)((run < 4 ? wr : wi) + (kb + 64 * (4 * tg + (run & 3))) * 578 + o * 16);
    }
    {
      const int id = u * 768 + tid;
      *(u32x4*)(ws + O_G + id * 16) = (u32x4){0u, 0u, 0u, 0u};
      if (id < 4608) *(u32x4*)(ws + O_WF + ((7 * (id / 288) + 6) * 18 + (id % 288 >> 4)) * 1024 + (12 + (id & 3) + 16 * ((id >> 2) & 3)) * 16) = (u32x4){0u, 0u, 0u, 0u};
    }
#pragma unroll
    for (int i = 0; i < 4; ++i) { int e = i * 768 + tid; e = e < 2312 ? e : 2311; *(u32x4*)(sm + e * 16) = v[i]; }
    __syncthreads();
    STAMP(0, 2);
#pragma unroll
    for (int i = 0; i < 3; ++i) {
      int e = i * 768 + tid, mm = e & 7, c = e >> 3;
      const char* p = sm + mm * 578 + 2 * (c + 1);
      u32x4 o;
#pragma unroll
      for (int r = 0; r < 4; ++r) o[r] = (unsigned)*(const u16*)(p + r * 4624) | ((unsigned)*(const u16*)(p + (4 + r) * 4624) << 16);
      *(u32x4*)(ws + O_WT + ((c * 4 + (kb >> 4)) * 2 + (tg >> 2)) * 1024 + (16 * (tg & 3) + (kb & 8) + mm) * 16) = o;
    }
    WAITV(0); PIN(code);
    FENCE(); STAMP(0, 5);
  }
}

extern "C" void kernel_launch(void* const* d_in, const int* in_sizes, int n_in, void* d_out, int out_size, void* d_ws,
                              size_t ws_size, hipStream_t stream) {
  const char *x0 = (const char*)d_in[0], *wr = (const char*)d_in[1], *wi = (const char*)d_in[2];
  const char *wqkv = (const char*)d_in[4], *wo = (const char*)d_in[5];
  char* ws = (char*)d_ws;
  (void)hipFuncSetAttribute((const void*)k_pre, hipFuncAttributeMaxDynamicSharedMemorySize, 73728);
  (void)hipFuncSetAttribute((const void*)k_fft1, hipFuncAttributeMaxDynamicSharedMemorySize, 73728);
  (void)hipFuncSetAttribute((const void*)k_attn0, hipFuncAttributeMaxDynamicSharedMemorySize, 133120);
  (void)hipFuncSetAttribute((const void*)k_attn1, hipFuncAttributeMaxDynamicSharedMemorySize, 133120);
  (void)hipFuncSetAttribute((const void*)k_out2, hipFuncAttributeMaxDynamicSharedMemorySize, 110592);
  (void)hipFuncSetAttribute((const void*)k_out3, hipFuncAttributeMaxDynamicSharedMemorySize, 110592);
  k_pre<<<310, 768, 73728, stream>>>(x0, wo, wr, wi, ws);
  k_fft1<<<252, 512, 73728, stream>>>(wqkv, ws);
  k_attn0<<<256, 512, 133120, stream>>>(ws);
  k_out2<<<192, 512, 110592, stream>>>(ws);
  k_fft2<<<252, 512, 40960, stream>>>(wo, ws);
  k_attn1<<<256, 512, 133120, stream>>>(ws);
  k_out3<<<192, 512, 110592, stream>>>(ws, (char*)d_out);
}
```

```cpp
#include <hip/hip_runtime.h>
#include <hip/hip_bf16.h>

typedef __bf16 bf16x8 __attribute__((ext_vector_type(8)));
typedef __bf16 bf16x2 __attribute__((ext_vector_type(2)));
typedef short s16x4 __attribute__((ext_vector_type(4)));
typedef float f32x4 __attribute__((ext_vector_type(4)));
typedef float f32x2 __attribute__((ext_vector_type(2)));
typedef unsigned u32x2 __attribute__((ext_vector_type(2)));
typedef unsigned u32x4 __attribute__((ext_vector_type(4)));
typedef unsigned short u16;
#define LDSP __attribute__((address_space(3)))
#define GLBP __attribute__((address_space(1)))
#define DI __device__ __forceinline__
#define FENCE() asm volatile("" ::: "memory")
#define WAITV(n) asm volatile("s_waitcnt vmcnt(" #n ")" ::: "memory")
#define SB() __builtin_amdgcn_sched_barrier(0)
#define BAR() do { SB(); asm volatile("s_waitcnt lgkmcnt(0)" ::: "memory"); __builtin_amdgcn_s_barrier(); FENCE(); SB(); } while (0)

#define STAMP(k, i) do {} while (0)
constexpr int S = 2048, D = 576;
constexpr bool ALIAS = false;
constexpr size_t XB = (size_t)S * D * 2;
constexpr size_t O_XH0 = 0, O_XH1 = XB, O_XH2 = ALIAS ? 0 : 2 * XB, O_XH3 = ALIAS ? XB : 3 * XB, O_OB = (ALIAS ? 2 : 4) * XB;
constexpr size_t O_WT = (ALIAS ? 3 : 6) * XB, O_X3 = ALIAS ? O_WT : 5 * XB;
constexpr size_t O_WOQ = ALIAS ? 4 * XB : 7 * XB + 663552, O_WOF = O_WOQ + 663552, O_WF = O_WOF + 663552;
constexpr size_t O_QTF = O_WF + 2064384, O_SS0 = O_QTF + 884736, O_SS2 = O_SS0 + 8192, O_G = O_SS2 + 6 * 8192;
constexpr int GP = 3072;
constexpr size_t GL = (size_t)256 * GP;

DI f32x4 mfma(bf16x8 a, bf16x8 b, f32x4 c) { return __builtin_amdgcn_mfma_f32_16x16x32_bf16(a, b, c, 0, 0, 0); }
DI unsigned pk2(float a, float b) { f32x2 f = {a, b}; return __builtin_bit_cast(unsigned, __builtin_convertvector(f, bf16x2)); }
DI bf16x8 pk8(f32x4 a, f32x4 b) { u32x4 u = {pk2(a[0], a[1]), pk2(a[2], a[3]), pk2(b[0], b[1]), pk2(b[2], b[3])}; return __builtin_bit_cast(bf16x8, u); }
DI float lo16(unsigned u) { return __builtin_bit_cast(float, u << 16); }
DI float hi16(unsigned u) { return __builtin_bit_cast(float, u & 0xffff0000u); }
DI float cosr(float rev) { return __builtin_amdgcn_cosf(__builtin_amdgcn_fractf(rev)); }
DI float sinr(float rev) { return __builtin_amdgcn_sinf(__builtin_amdgcn_fractf(rev)); }
DI void glds(const char* g, char* l) { __builtin_amdgcn_global_load_lds((const GLBP void*)g, (LDSP void*)l, 16, 0, 0); }
DI bf16x8 ldf(const char* p) { return *(const bf16x8*)p; }
typedef __amdgpu_buffer_rsrc_t rsrc_t;
DI rsrc_t mkrs(const void* p, unsigned bytes) { return __builtin_amdgcn_make_buffer_rsrc((void*)p, 0, bytes, 0x00020000); }
DI void bglds(rsrc_t r, char* l, int voff, int soff) { __builtin_amdgcn_raw_ptr_buffer_load_lds(r, (LDSP void*)l, 16, voff, soff, 0, 0); }
#define ALD16(dst, voff, sbase, imm) asm volatile("global_load_dwordx4 %0, %1, %2 offset:" #imm : "=v"(dst) : "v"(voff), "s"(sbase))
#define ALD8(dst, voff, sbase) asm volatile("global_load_dwordx2 %0, %1, %2" : "=v"(dst) : "v"(voff), "s"(sbase))
#define PIN(x) asm volatile("" : "+v"(x))
DI bf16x8 trf(const char* p, int d) {
  s16x4 lo = __builtin_amdgcn_ds_read_tr16_b64_v4i16((LDSP s16x4*)p), hi = __builtin_amdgcn_ds_read_tr16_b64_v4i16((LDSP s16x4*)(p + d));
  return __builtin_bit_cast(bf16x8, __builtin_shufflevector(lo, hi, 0, 1, 2, 3, 4, 5, 6, 7));
}
DI u32x2 pk4(f32x4 v) { return (u32x2){pk2(v[0], v[1]), pk2(v[2], v[3])}; }
constexpr u32x4 ONES = {0x3f803f80u, 0x3f803f80u, 0x3f803f80u, 0x3f803f80u};
constexpr f32x4 Z4 = {0.f, 0.f, 0.f, 0.f};
DI float qent(int d, int j) {
  int f = (j >> 1) + 1; float ph = (j & 1) ? 0.25f : 0.f, sc = 0.05892556509887896f;
  if (j >= 574) { sc = 0.041666666666666664f; ph = 0.f; f = (j == 575) ? 0 : 288; }
  return sc * cosr((float)(f * d) * (1.f / 576.f) + ph);
}
DI int operm(int k) {
  int hp = k / 36, c = k - 36 * hp, p = c >> 2, i = c & 3;
  return p < 8 ? 18 * (2 * hp + (p >> 2)) + 4 * (p & 3) + i : 18 * (2 * hp + (i >> 1)) + 16 + (i & 1);
}

DI void qgen(bf16x8* qf, int j, int d0, int dd) {
  int f = (j >> 1) + 1; float ph = (j & 1) ? 0.25f : 0.f, sc = 0.05892556509887896f;
  if (j >= 574) { sc = 0.041666666666666664f; ph = 0.f; f = (j == 575) ? 0 : 288; }
  const float st = (float)f * (1.f / 576.f), c2 = 2.f * __builtin_amdgcn_cosf(st), p0 = st * (float)d0 + ph;
#pragma unroll
  for (int i = 0; i < 9; ++i) {
    float p = __builtin_amdgcn_fractf(st * (float)(dd * i) + p0), v[8];
    v[0] = sc * __builtin_amdgcn_cosf(p); v[1] = sc * __builtin_amdgcn_cosf(p + st);
#pragma unroll
    for (int e = 2; e < 8; ++e) v[e] = c2 * v[e - 1] - v[e - 2];
    qf[i] = __builtin_bit_cast(bf16x8, (u32x4){pk2(v[0], v[1]), pk2(v[2], v[3]), pk2(v[4], v[5]), pk2(v[6], v[7])});
    PIN(qf[i]);
  }
}
template <int MODE>
DI void gemm_q(char* sm, int rb, int ng, const char* xs, char* out, float* ssq) {
  const int tid = threadIdx.x, w = __builtin_amdgcn_readfirstlane(tid >> 6), l = tid & 63, g = l >> 4, m = l & 15;
  const int kh = w >= 6, nt = w - 6 * kh, jt = 6 * ng + nt;
  STAMP(0, 0);
  {
    const rsrc_t xrs = mkrs(xs, MODE == 0 ? (unsigned)XB : 663552u);
    int voff[2];
#pragma unroll
    for (int h = 0; h < 2; ++h) {
      int r = 64 * rb + 8 * ((w + 4 * h) & 7) + (l >> 3);
      voff[h] = (MODE == 1 ? operm(r) : r) * 1152 + (((l & 7) ^ (l >> 3)) << 4);
    }
#pragma unroll
    for (int i = 0; i < 6; ++i) { int q = w + 12 * i; bglds(xrs, sm + q * 1024, voff[i & 1], (q >> 3) * 128); }
  }
  SB(); STAMP(0, 1);
  bf16x8 qf[9];
  qgen(qf, 16 * jt + m, 8 * g + 32 * kh, 64);
  WAITV(0); BAR(); STAMP(0, 2);
  f32x4 acc[4] = {}, gr = Z4, su = Z4;
  const bool stats = MODE == 0 && ng == 0 && nt < 4;
  const int x2 = 2 * kh;
  const char* xb = sm + (m >> 3) * 1024 + (m & 7) * 128 + (((4 * kh + g) ^ (m & 7)) << 4);
#pragma unroll
  for (int i = 0; i < 9; ++i) {
#pragma unroll
    for (int a = 0; a < 4; ++a) {
      bf16x8 xf = ldf(xb + i * 8192 + (a ^ x2) * 2048);
      acc[a] = MODE == 1 ? mfma(xf, qf[i], acc[a]) : mfma(qf[i], xf, acc[a]);
    }
    if (stats) { bf16x8 xf = ldf(xb + i * 8192 + nt * 2048); gr = mfma(xf, xf, gr); su = mfma(__builtin_bit_cast(bf16x8, ONES), xf, su); }
  }
  BAR(); STAMP(0, 4);
  *(f32x4*)(sm + w * 4096 + l * 16) = acc[2]; *(f32x4*)(sm + w * 4096 + 1024 + l * 16) = acc[3];
  if (stats && kh) { *(f32x4*)(sm + w * 4096 + 2048 + l * 16) = gr; *(f32x4*)(sm + w * 4096 + 3072 + l * 16) = su; }
  BAR();
  const char* pp = sm + (kh ? w - 6 : w + 6) * 4096 + l * 16;
#pragma unroll
  for (int a = 0; a < 2; ++a) {
    f32x4 v = acc[a] + *(const f32x4*)(pp + a * 1024);
    const int rt = 4 * rb + x2 + a, n = 16 * jt + 4 * g;
    if (MODE == 1) *(u32x2*)(out + (jt * 18 + (rt >> 1)) * 1024 + (m + 16 * (2 * (rt & 1) + (g >> 1))) * 16 + (g & 1) * 8) = pk4(v);
    else *(u32x2*)(out + ((n >> 3) * S + 16 * rt + m) * 16 + (n & 4) * 2) = pk4(v);
  }
  if (stats && !kh) {
    gr += *(const f32x4*)(pp + 2048); su += *(const f32x4*)(pp + 3072);
    float dsel = (m & 2) ? ((m & 1) ? gr[3] : gr[2]) : ((m & 1) ? gr[1] : gr[0]);
    if ((m >> 2) == g) ssq[64 * rb + 16 * nt + m] = fmaxf(dsel - su[0] * su[0] * (1.f / 576.f), 0.f);
  }
  FENCE(); STAMP(0, 5);
}

template <int NP>
DI void fft_cols(char* sm, int b, const char* xin, char* xout, const float* ssq, const char* wt) {
  const int tid = threadIdx.x, w8 = __builtin_amdgcn_readfirstlane(tid >> 6), l = tid & 63, g = l >> 4, m = l & 15;
  const int w = w8 & 3, hi = w8 >> 2;
  const int kb = (b & 7) + 8 * (b >> 4), half = (b >> 3) & 1;
  const bool last = kb == 71 && half == 1;
  constexpr int KID = NP == 1 ? 1 : 4;
  STAMP(KID, 0);
  f32x4 ps[NP];
#pragma unroll
  for (int p = 0; p < NP; ++p) ps[p] = *(const f32x4*)(ssq + p * S + tid * 4);
  u32x2 xv[4];
  const int xo = (kb * S + 32 * (16 * w + 4 * g) + 16 * hi + m) * 16 + 8 * half;
#pragma unroll
  for (int r = 0; r < 4; ++r) xv[r] = *(const u32x2*)(xin + xo + r * 512);
  u32x4 wv[2];
#pragma unroll
  for (int t = 0; t < 2; ++t) wv[t] = *(const u32x4*)(wt + (((4 * kb + 2 * half + hi) * 4 + w) * 2 + t) * 1024 + l * 16);
  FENCE(); STAMP(KID, 1);
  SB();
  bf16x8 c64[2], s64[2], n64[2], c32[2], s32[2], n32[2];
  f32x4 t1c[2], t1s[2], t2c[2], t2s[2];
  {
    const float k1 = (float)(16 * w + m), g4 = (float)(4 * g);
    const unsigned sg = (m & 1) ? 0x80008000u : 0u;
    float c[8], sn[8];
    const float a = k1 * (1.f / 64.f), a2 = 2.f * __builtin_amdgcn_cosf(a), p = a * (g4 + g4);
    c[0] = __builtin_amdgcn_cosf(p); c[1] = __builtin_amdgcn_cosf(p + a); sn[0] = __builtin_amdgcn_sinf(p); sn[1] = __builtin_amdgcn_sinf(p + a);
#pragma unroll
    for (int j = 2; j < 8; ++j) { c[j] = a2 * c[j - 1] - c[j - 2]; sn[j] = a2 * sn[j - 1] - sn[j - 2]; }
    u32x4 uc = {pk2(c[0], c[1]), pk2(c[2], c[3]), pk2(c[4], c[5]), pk2(c[6], c[7])}, us = {pk2(sn[0], sn[1]), pk2(sn[2], sn[3]), pk2(sn[4], sn[5]), pk2(sn[6], sn[7])};
    c64[0] = __builtin_bit_cast(bf16x8, uc); c64[1] = __builtin_bit_cast(bf16x8, uc ^ sg);
    s64[0] = __builtin_bit_cast(bf16x8, us); s64[1] = __builtin_bit_cast(bf16x8, us ^ sg);
    n64[0] = __builtin_bit_cast(bf16x8, us ^ 0x80008000u); n64[1] = __builtin_bit_cast(bf16x8, us ^ (sg ^ 0x80008000u));
    const float e = (float)m * (1.f / 32.f);
#pragma unroll
    for (int j = 0; j < 4; ++j) { float q = e * (g4 + (float)j); c[j] = __builtin_amdgcn_cosf(q); sn[j] = __builtin_amdgcn_sinf(q); }
    unsigned c0 = pk2(c[0], c[1]), c1 = pk2(c[2], c[3]), s0 = pk2(sn[0], sn[1]), s1 = pk2(sn[2], sn[3]);
    uc = (u32x4){c0, c1, c0 ^ sg, c1 ^ sg}; us = (u32x4){s0, s1, s0 ^ sg, s1 ^ sg};
    c32[0] = __builtin_bit_cast(bf16x8, uc); c32[1] = __builtin_bit_cast(bf16x8, uc ^ 0x80000000u);
    s32[0] = __builtin_bit_cast(bf16x8, us); s32[1] = __builtin_bit_cast(bf16x8, us ^ 0x80000000u);
    n32[0] = __builtin_bit_cast(bf16x8, us ^ 0x80008000u); n32[1] = __builtin_bit_cast(bf16x8, us ^ 0x00008000u);
    const float b1 = k1 * (1.f / 2048.f), b12 = 2.f * __builtin_amdgcn_cosf(b1);
#pragma unroll
    for (int h = 0; h < 2; ++h) {
      const float p1 = b1 * (g4 + (float)(16 * h)), b2 = (float)(16 * h + m) * (1.f / 2048.f), b22 = 2.f * __builtin_amdgcn_cosf(b2), p2 = b2 * (g4 + (float)(16 * w));
      t1c[h][0] = __builtin_amdgcn_cosf(p1); t1c[h][1] = __builtin_amdgcn_cosf(p1 + b1); t1s[h][0] = __builtin_amdgcn_sinf(p1); t1s[h][1] = __builtin_amdgcn_sinf(p1 + b1);
      t2c[h][0] = __builtin_amdgcn_cosf(p2); t2c[h][1] = __builtin_amdgcn_cosf(p2 + b2); t2s[h][0] = __builtin_amdgcn_sinf(p2); t2s[h][1] = __builtin_amdgcn_sinf(p2 + b2);
#pragma unroll
      for (int r = 2; r < 4; ++r) {
        t1c[h][r] = b12 * t1c[h][r - 1] - t1c[h][r - 2]; t1s[h][r] = b12 * t1s[h][r - 1] - t1s[h][r - 2];
        t2c[h][r] = b22 * t2c[h][r - 1] - t2c[h][r - 2]; t2s[h][r] = b22 * t2s[h][r - 1] - t2s[h][r - 2];
      }
      PIN(c64[h]); PIN(s64[h]); PIN(n64[h]); PIN(c32[h]); PIN(s32[h]); PIN(n32[h]); PIN(t1c[h]); PIN(t1s[h]); PIN(t2c[h]); PIN(t2s[h]);
    }
  }
  SB();
  FENCE(); STAMP(KID, 2);
  float* rsl = (float*)(sm + 32768);
  {
    f32x4 a = ps[0];
#pragma unroll
    for (int p = 1; p < NP; ++p) a += ps[p];
#pragma unroll
    for (int i = 0; i < 4; ++i) a[i] = __builtin_amdgcn_rsqf(a[i] * (1.f / 576.f) + 1e-5f) * (1.f / 2048.f);
    *(f32x4*)(rsl + tid * 4) = a;
  }
  BAR();
  const int wo = m * 128 + (((2 * w + (g >> 1)) ^ (m & 7)) << 4) + (g & 1) * 8;
  {
    f32x4 u[4];
#pragma unroll
    for (int r = 0; r < 4; ++r) {
      float rs = rsl[32 * (16 * w + 4 * g + r) + 16 * hi + m];
      u[0][r] = lo16(xv[r][0]) * rs; u[1][r] = hi16(xv[r][0]) * rs; u[2][r] = lo16(xv[r][1]) * rs;
      u[3][r] = last ? 0.f : hi16(xv[r][1]) * rs;
    }
#pragma unroll
    for (int p = 0; p < 4; ++p) *(u32x2*)(sm + p * 4096 + hi * 2048 + wo) = pk4(u[p]);
  }
  BAR(); STAMP(KID, 3);
  const int ro0 = m * 128 + ((g ^ (m & 7)) << 4), ro1 = ro0 ^ 64;
  {
    f32x4 re[2], im[2];
#pragma unroll
    for (int t = 0; t < 2; ++t) {
      const char* p = sm + hi * 8192 + t * 2048;
      bf16x8 a0 = ldf(p + ro0), a1 = ldf(p + ro1), b0 = ldf(p + 4096 + ro0), b1 = ldf(p + 4096 + ro1);
      f32x4 x = mfma(a0, c64[0], Z4), y = mfma(b0, c64[0], Z4);
      x = mfma(a1, c64[1], x); y = mfma(b1, c64[1], y);
      x = mfma(b0, s64[0], x); y = mfma(a0, n64[0], y);
      x = mfma(b1, s64[1], x); y = mfma(a1, n64[1], y);
#pragma unroll
      for (int r = 0; r < 4; ++r) { re[t][r] = x[r] * t1c[t][r] + y[r] * t1s[t][r]; im[t][r] = y[r] * t1c[t][r] - x[r] * t1s[t][r]; }
    }
    bf16x8 fr = pk8(re[0], re[1]), fi = pk8(im[0], im[1]);
#pragma unroll
    for (int t = 0; t < 2; ++t) {
      f32x4 x = mfma(c32[t], fr, Z4), y = mfma(c32[t], fi, Z4);
      x = mfma(s32[t], fi, x); y = mfma(n32[t], fr, y);
#pragma unroll
      for (int r = 0; r < 4; ++r) {
        float a = lo16(wv[t][r]), bb = hi16(wv[t][r]);
        re[t][r] = x[r] * a - y[r] * bb; im[t][r] = x[r] * bb + y[r] * a;
      }
    }
    fr = pk8(re[0], re[1]); fi = pk8(im[0], im[1]);
#pragma unroll
    for (int t = 0; t < 2; ++t) {
      f32x4 x = mfma(fr, c32[t], Z4), y = mfma(fi, c32[t], Z4);
      x = mfma(fi, n32[t], x); y = mfma(fr, s32[t], y);
      f32x4 vr, vi;
#pragma unroll
      for (int r = 0; r < 4; ++r) { vr[r] = x[r] * t2c[t][r] - y[r] * t2s[t][r]; vi[r] = y[r] * t2c[t][r] + x[r] * t2s[t][r]; }
      char* p = sm + 16384 + hi * 8192 + t * 2048 + wo;
      *(u32x2*)p = pk4(vr); *(u32x2*)(p + 4096) = pk4(vi);
    }
  }
  BAR(); STAMP(KID, 4);
  {
    f32x4 yr[2], yi[2];
#pragma unroll
    for (int c = 0; c < 2; ++c) {
      const char* p = sm + 16384 + c * 8192 + hi * 2048;
      bf16x8 a0 = ldf(p + ro0), a1 = ldf(p + ro1), b0 = ldf(p + 4096 + ro0), b1 = ldf(p + 4096 + ro1);
      f32x4 x = mfma(c64[0], a0, Z4), y = mfma(c64[0], b0, Z4);
      x = mfma(c64[1], a1, x); y = mfma(c64[1], b1, y);
      x = mfma(n64[0], b0, x); y = mfma(s64[0], a0, y);
      x = mfma(n64[1], b1, x); y = mfma(s64[1], a1, y);
      yr[c] = x; yi[c] = y;
    }
#pragma unroll
    for (int r = 0; r < 4; ++r) {
      u32x2 o = {pk2(lo16(xv[r][0]) + yr[0][r], hi16(xv[r][0]) + yi[0][r]),
                 pk2(lo16(xv[r][1]) + yr[1][r], hi16(xv[r][1]) + (last ? 0.f : yi[1][r]))};
      *(u32x2*)(xout + xo + r * 512) = o;
    }
  }
  FENCE(); STAMP(KID, 5);
}

DI void wqkv_rot(char* sm, int u, const char* wqkv, char* wf) {
  const int tid = threadIdx.x, w = __builtin_amdgcn_readfirstlane(tid >> 6), l = tid & 63, g = l >> 4, m = l & 15;
  const int jg = u & 3, ng = u >> 2, kh = w >> 2, wc = w & 3, jt = 9 * jg + 2 * wc + kh, j8 = 9 * jg + 8;
  STAMP(1, 0);
  const rsrc_t rs = mkrs(wqkv, 1990656u);
  const int voff = (l >> 3) * 3456 + (((((l >> 1) & 3) ^ (((l >> 4) & 1) | ((w & 1) << 1))) << 5) | ((l & 1) << 4));
  const int l16 = l * 16;
#pragma unroll
  for (int i = 0; i < 9; ++i) bglds(rs, sm + (w + 8 * i) * 1024, voff, (w + 8 * i) * 27648 + ng * 128);
  SB();
  bf16x8 af[3][9];
  qgen(af[0], 16 * jt + m, 288 * kh + 8 * g, 32);
  qgen(af[1], 16 * (jt + 1 - 2 * kh) + m, 288 * kh + 8 * g, 32);
  qgen(af[2], 16 * j8 + m, 288 * kh + 8 * g, 32);
  int ob[4];
#pragma unroll
  for (int t = 0; t < 4; ++t) {
    int n = 64 * ng + 16 * (t ^ wc) + m, seg = (n >= 576) + (n >= 1152), c = n - 576 * seg, hp = c / 36;
    int cc = c - 36 * hp + (seg == 0 ? 72 : seg == 1 ? 0 : 36);
    ob[t] = (7 * hp + (cc >> 4)) * 18432 + (cc & 15) * 16;
  }
  STAMP(1, 2);
  WAITV(0); BAR(); STAMP(1, 3);
  f32x4 acc[2][4] = {}, a8 = Z4;
  const int rbase = 9 * kh * 4096 + (8 * g + (m >> 2)) * 128 + 8 * (m & 3), fx = (((m >> 3) & 1) | ((g & 1) << 1)) ^ wc;
#pragma unroll
  for (int i = 0; i < 9; ++i) {
#pragma unroll
    for (int t = 0; t < 4; ++t) {
      bf16x8 bfr = trf(sm + i * 4096 + rbase + ((t ^ fx) << 5), 512);
      acc[0][t] = mfma(af[0][i], bfr, acc[0][t]); acc[1][t] = mfma(af[1][i], bfr, acc[1][t]);
      if (t == 0) a8 = mfma(af[2][i], bfr, a8);
    }
  }
  BAR(); STAMP(1, 4);
#pragma unroll
  for (int t = 0; t < 4; ++t) *(f32x4*)(sm + (w * 5 + t) * 1024 + l16) = acc[1][t];
  *(f32x4*)(sm + (w * 5 + 4) * 1024 + l16) = a8;
  BAR();
  const int jo = (jt >> 1) * 1024 + (2 * (jt & 1) + (g >> 1)) * 256 + (g & 1) * 8;
#pragma unroll
  for (int t = 0; t < 4; ++t) *(u32x2*)(wf + ob[t] + jo) = pk4(acc[0][t] + *(const f32x4*)(sm + ((w ^ 4) * 5 + t) * 1024 + l16));
  if (!kh) {
    f32x4 o = a8 + *(const f32x4*)(sm + ((w ^ 4) * 5 + 4) * 1024 + l16);
    if (j8 == 35 && g == 3) o[3] = 0.f;
    *(u32x2*)(wf + ob[0] + (j8 >> 1) * 1024 + (2 * (j8 & 1) + (g >> 1)) * 256 + (g & 1) * 8) = pk4(o);
  }
  FENCE(); STAMP(1, 5);
}

extern "C" __global__ void __launch_bounds__(512, 2) k_fft1(const char* wqkv, char* ws) {
  extern __shared__ __attribute__((aligned(16))) char sm[];
  const int b = blockIdx.x;
  if (b < 144) fft_cols<1>(sm, b, ws + O_XH0, ws + O_XH1, (const float*)(ws + O_SS0), ws + O_WT);
  else wqkv_rot(sm, b - 144, wqkv, ws + O_WF);
}
__global__ void __launch_bounds__(512, 2) k_fft2(const char* wo, char* ws) {
  extern __shared__ __attribute__((aligned(16))) char sm[];
  const int b = blockIdx.x;
  if (b < 144) fft_cols<6>(sm, b, ws + O_XH2, ws + O_XH3, (const float*)(ws + O_SS2), ws + O_WT);
  else {
    const int e = (b - 144) * 512 + threadIdx.x, l = e & 63, pc = e >> 6, nt = pc / 18, ks = pc - 18 * nt;
    unsigned h[8];
    if (e < 41472) {
      const int r0 = operm(32 * ks + 8 * (l >> 4)), r1 = operm(32 * ks + 8 * (l >> 4) + 4), c0 = (32 * ks + 8 * (l >> 4)) % 36;
#pragma unroll
      for (int j = 0; j < 8; ++j) {
        int r = (j < 4 ? r0 : r1) + (c0 == (j < 4 ? 32 : 28) ? 18 * ((j >> 1) & 1) + (j & 1) : (j & 3));
        h[j] = *(const u16*)(wo + r * 1152 + (16 * nt + (l & 15)) * 2);
      }
    }
    if (e < (int)(GL / 16)) *(u32x4*)(ws + O_G + GL + e * 16) = (u32x4){0u, 0u, 0u, 0u};
    const int hp = nt / 3, c = 16 * (nt - 3 * hp) + (l & 15), d = 36 * hp + (c < 36 ? c : 0), j0 = 32 * ks + 8 * (l >> 4);
    *(u32x4*)(ws + O_QTF + e * 16) = (u32x4){pk2(qent(d, j0), qent(d, j0 + 1)), pk2(qent(d, j0 + 2), qent(d, j0 + 3)),
                                             pk2(qent(d, j0 + 4), qent(d, j0 + 5)), pk2(qent(d, j0 + 6), qent(d, j0 + 7))};
    if (e < 41472) *(u32x4*)(ws + O_WOF + e * 16) = (u32x4){h[0] | (h[1] << 16), h[2] | (h[3] << 16), h[4] | (h[5] << 16), h[6] | (h[7] << 16)};
  }
}

constexpr int A_QT = 103936, A_KV = A_QT + 55296;
constexpr int A_PK = 65536, A_V = A_PK + 13056, A_Q = A_V + 13056;
template <int LAST>
DI void attn_body(const char* xin, char* ws) {
  extern __shared__ __attribute__((aligned(16))) char sm[];
  const int tid = threadIdx.x, w = __builtin_amdgcn_readfirstlane(tid >> 6), l = tid & 63, g = l >> 4, m = l & 15;
  const int b = blockIdx.x, rb = 4 * ((b & 7) >> 1) + ((b >> 3) & 3), hp = 8 * (b & 1) + (b >> 5);
  const int wr = w & 3, kh = w >> 2, s0 = 128 * rb + 32 * wr;
  constexpr int KID = LAST ? 5 : 2;
  STAMP(KID, 0);
  const rsrc_t wrs = mkrs(ws + O_WF + hp * 129024, 129024);
  const int l16 = l * 16, xo0 = (g * S + s0 + 16 * kh + m) * 16, xo1 = xo0 ^ 256;
  bf16x8 xf[2][9];
  auto issue1 = [&](int j, int p) {
    if (p < 2) {
      int q = w + 8 * p; q = q < 14 ? q : q - 8;
      int pc = (q >> 1) * 18 + 2 * j + (q & 1);
      bglds(wrs, sm + pc * 1024, l16, pc * 1024);
    } else {
      const char* xb = xin + (2 * j + kh) * (4 * S * 16);
      if (p == 2) ALD16(xf[0][j], xo0, xb, 0); else ALD16(xf[1][j], xo1, xb, 0);
    }
    FENCE(); SB();
  };
  auto issue = [&](int j) { issue1(j, 0); issue1(j, 1); issue1(j, 2); issue1(j, 3); };
#pragma unroll
  for (int j = 0; j < 3; ++j) issue(j);
  if (tid < 256) *(u32x4*)(sm + A_KV + tid * 16) = (u32x4){0u, 0u, 0u, 0u};
  const int pa = w < 6 ? w / 3 : 2, pb = w < 6 ? w - 3 * pa : w - 5;
  const int dq = 4 * pa + g, ec = 16 * pb + m - 4, eh = ec >= 18;
  const bool isz = ec == 36, valid = isz ? dq < 9 : (ec >= 0 && ec < 36 && dq >= 4 * eh && dq <= 4 * eh + 4);
  const int slot = (isz ? 180 + dq : 36 * (dq - 4 * eh) + ec) * 16;
  STAMP(KID, 1);
  f32x4 acc[2][7] = {};
  float sq[2] = {0.f, 0.f};
  bf16x8 wf[2][7];
  WAITV(8); BAR(); STAMP(KID, 2);
#pragma unroll
  for (int t = 0; t < 7; ++t) wf[0][t] = ldf(sm + (t * 18 + kh) * 1024 + l16);
  issue(3);
#pragma unroll
  for (int i = 0; i < 9; ++i) {
    if (i < 8) {
      if (i <= 5) WAITV(8); else if (i == 6) WAITV(4); else WAITV(0);
      BAR();
    }
    PIN(xf[0][i]); PIN(xf[1][i]);
#pragma unroll
    for (int t = 0; t < 7; ++t) {
      acc[0][t] = mfma(xf[0][i], wf[i & 1][t], acc[0][t]); acc[1][t] = mfma(xf[1][i], wf[i & 1][t], acc[1][t]);
      if (t < 4) {
#pragma unroll
        for (int a = 0; a < 2; ++a) {
          unsigned u = __builtin_bit_cast(u32x4, xf[a][i])[t];
          if (i == 8 && t == 3) u = (g == 3 && kh) ? (u & 0xffffu) : u;
          bf16x2 h = __builtin_bit_cast(bf16x2, u);
          sq[a] = __builtin_amdgcn_fdot2_f32_bf16(h, h, sq[a], false);
        }
      }
      SB();
      if (i < 8) wf[(i + 1) & 1][t] = ldf(sm + (t * 18 + 2 * i + 2 + kh) * 1024 + l16);
      SB();
      if (!(t & 1) && i + 4 < 9) issue1(i + 4, t >> 1);
    }
  }
  BAR(); STAMP(KID, 3);
  if (LAST) {
    const rsrc_t qrs = mkrs(ws + O_QTF + hp * 55296, 55296);
#pragma unroll
    for (int i = 0; i < 7; ++i) { int q = w + 8 * i; q = q < 54 ? q : q - 8; bglds(qrs, sm + A_QT + q * 1024, l16, q * 1024); }
  }
  SB();
#pragma unroll
  for (int t = 0; t < 7; ++t) *(f32x4*)(sm + (w * 8 + t) * 1024 + l16) = acc[1][t];
  *(float*)(sm + (w * 8 + 7) * 1024 + l * 4) = sq[1];
  BAR();
  float rq = sq[0] + *(const float*)(sm + ((w ^ 4) * 8 + 7) * 1024 + l * 4);
  f32x4 v[7];
#pragma unroll
  for (int t = 0; t < 7; ++t) v[t] = acc[0][t] + *(const f32x4*)(sm + ((w ^ 4) * 8 + t) * 1024 + l16);
  rq += __shfl_xor(rq, 16); rq += __shfl_xor(rq, 32);
  rq = __builtin_amdgcn_rsqf(rq * (1.f / 576.f) + 1e-5f);
  {
    f32x4 rs, rl;
#pragma unroll
    for (int r = 0; r < 4; ++r) { rs[r] = __shfl(rq, 4 * g + r); rl[r] = rs[r] * 1.4426950408889634f; }
    const int so = (32 * wr + 16 * kh + 4 * g) * 2 + m * 272;
#pragma unroll
    for (int t = 0; t < 7; ++t) {
      if (t != 3) {
        f32x4 p;
        const f32x4 z = v[t] * rl, y = __builtin_elementwise_fma(v[t], rs, (f32x4){1e-6f, 1e-6f, 1e-6f, 1e-6f});
        f32x4 e;
#pragma unroll
        for (int r = 0; r < 4; ++r) { e[r] = __builtin_amdgcn_fmed3f(__builtin_amdgcn_exp2f(z[r]), 0.f, 1.f); p[r] = fmaxf(y[r], 1e-6f); }
        p += e;
        if (t < 3) *(u32x2*)(sm + A_PK + t * 4352 + so) = pk4(p);
        else *(u32x2*)(sm + A_Q + w * 1536 + (16 * (t - 4) + m) * 32 + g * 8) = pk4(p);
      }
      if (t >= 2 && t <= 4) {
        u32x2 u = pk4(v[t] * rs);
        if (t == 4 && m == 8) u = (u32x2){0x3f803f80u, 0x3f803f80u};
        *(u32x2*)(sm + A_V + (t - 2) * 4352 + so) = u;
      }
    }
  }
  BAR(); STAMP(KID, 4);
  const rsrc_t grs = mkrs(ws + O_G + (LAST ? GL : 0) + hp * 16 * GP, 16 * GP);
  {
    f32x4 c = Z4;
#pragma unroll
    for (int k = 0; k < 4; ++k)
      c = mfma(ldf(sm + A_PK + pa * 4352 + m * 272 + k * 64 + g * 16), ldf(sm + A_V + pb * 4352 + m * 272 + k * 64 + g * 16), c);
    u32x4 u = ((__builtin_bit_cast(u32x4, c) + 0x26u) & 0xffffff00u) | 0x5au;
    if (valid) __builtin_amdgcn_raw_buffer_store_b128(u, grs, slot + rb * GP, 0, 16);
  }
  FENCE();
  u32x4 r[16];
  u32x2 x3v[3];
  const int sf = __shfl(slot, __ffsll((unsigned long long)__ballot(valid)) - 1), sl = valid ? slot : sf;
  if (!LAST) {
    WAITV(0);
#pragma unroll
    for (int p = 0; p < 16; ++p) r[p] = __builtin_amdgcn_raw_buffer_load_b128(grs, sl, p * GP, 16);
  }
  if (LAST) {
    WAITV(1); BAR();
    f32x4 y[2][3] = {};
#pragma unroll
    for (int i = 0; i < 9; ++i)
#pragma unroll
      for (int t = 0; t < 3; ++t) {
        bf16x8 qf = ldf(sm + A_QT + (t * 18 + 2 * i + kh) * 1024 + l16);
        y[0][t] = mfma(qf, xf[0][i], y[0][t]); y[1][t] = mfma(qf, xf[1][i], y[1][t]);
      }
    SB();
#pragma unroll
    for (int p = 0; p < 16; ++p) r[p] = __builtin_amdgcn_raw_buffer_load_b128(grs, sl, p * GP, 0);
    SB();
    char* ex = sm + A_PK;
#pragma unroll
    for (int t = 0; t < 3; ++t) *(f32x4*)(ex + (w * 3 + t) * 1024 + l16) = y[1][t];
    BAR();
#pragma unroll
    for (int t = 0; t < 3; ++t) {
      x3v[t] = pk4(y[0][t] + *(const f32x4*)(ex + ((w ^ 4) * 3 + t) * 1024 + l16));
      PIN(x3v[t]);
    }
  }
  FENCE();
  {
    for (int spin = 0; spin < 1000000; ++spin) {
      bool bad = false;
#pragma unroll
      for (int p = 0; p < 16; ++p)
        if (!__all(((r[p][0] & r[p][1]) & (r[p][2] & r[p][3]) & 0xffu) == 0x5au)) {
          FENCE();
          r[p] = __builtin_amdgcn_raw_buffer_load_b128(grs, sl, p * GP, 16);
          bad = true;
        }
      if (!bad) break;
    }
    if (valid) {
      f32x4 sum = Z4;
#pragma unroll
      for (int p = 0; p < 16; ++p) sum += __builtin_bit_cast(f32x4, r[p]);
      u32x2 u = pk4(sum);
      if (isz ? dq <= 4 : !eh) *(u32x2*)(sm + A_KV + (isz ? 18 : ec) * 64 + 16 + 8 * dq) = (u32x2){u[0], dq == 4 ? 0u : u[1]};
      if (isz ? dq >= 4 : eh) *(u32x2*)(sm + A_KV + (isz ? 50 : ec + 14) * 64 - 16 + 8 * dq) = (u32x2){dq == 4 ? 0u : u[0], u[1]};
    }
  }
  if (LAST) {
#pragma unroll
    for (int t = 0; t < 3; ++t)
      if (t < 2 || g == 0) *(u32x2*)(ws + O_X3 + (s0 + 16 * kh + m) * 1152 + (36 * hp + 16 * t + 4 * g) * 2) = x3v[t];
  }
  BAR(); STAMP(KID, 7);
  {
    unsigned tail[2];
    char* ob = ws + O_OB + (s0 + 16 * kh + m) * 16;
#pragma unroll
    for (int h = 0; h < 2; ++h) {
      bf16x8 k0 = ldf(sm + A_KV + (32 * h + m) * 64 + g * 16), k1 = ldf(sm + A_KV + (32 * h + 16 + m) * 64 + g * 16);
      bf16x8 pf = trf(sm + A_Q + w * 1536 + (16 * h + 8 * g + (m >> 2)) * 32 + (m & 3) * 8, 128);
      f32x4 n0 = mfma(k0, pf, Z4), n1 = mfma(k1, pf, Z4);
      float inv = __builtin_amdgcn_rcpf(__shfl(n1[2], m) + 1e-6f);
      int P = 9 * hp + 4 * h + g;
      *(u32x2*)(ob + (P >> 1) * (S * 16) + (P & 1) * 8) = pk4(n0 * inv);
      tail[h] = pk2(n1[0] * inv, n1[1] * inv);
    }
    int P = 9 * hp + 8;
    if (g == 0) *(u32x2*)(ob + (P >> 1) * (S * 16) + (P & 1) * 8) = (u32x2){tail[0], tail[1]};
  }
  FENCE(); STAMP(KID, 8);
}

template <int MODE>
DI void out_body(char* ws, char* outp) {
  extern __shared__ __attribute__((aligned(16))) char sm[];
  const int tid = threadIdx.x, w = __builtin_amdgcn_readfirstlane(tid >> 6), l = tid & 63, g = l >> 4, m = l & 15;
  const int b = blockIdx.x, rb = 8 * ((b & 7) >> 1) + ((b >> 3) & 7), ng = 3 * (b & 1) + (b >> 6);
  const int wr = w & 1, ch = (w >> 1) & 1, kh = w >> 2, s = 64 * rb + 32 * wr + 16 * kh + m;
  constexpr int KID = MODE == 2 ? 3 : 6;
  STAMP(KID, 0);
  const rsrc_t wrs = mkrs(ws + (MODE == 2 ? O_WOQ : O_WOF) + ng * 110592, 110592);
  const int l16 = l * 16, xo0 = (g * S + s) * 16, xo1 = xo0 ^ 256;
  bf16x8 xf[2][9];
  auto issue1 = [&](int st, int p) {
    if (p < 3) {
      if (st == 4 && p == 2) return;
      int q = w + 8 * p; if (st == 4) q = q < 12 ? q : q - 8;
      int pc = st < 4 ? (q >> 2) * 18 + 4 * st + (q & 3) : (q >> 1) * 18 + 16 + (q & 1);
      bglds(wrs, sm + pc * 1024, l16, pc * 1024);
    } else {
      const int i = 2 * st + ((p - 3) >> 1);
      if (i > 8) return;
      const char* xb = ws + O_OB + (2 * i + kh) * (4 * S * 16);
      if (p & 1) ALD16(xf[0][i], xo0, xb, 0); else ALD16(xf[1][i], xo1, xb, 0);
    }
    FENCE(); SB();
  };
  auto issue = [&](int st) {
#pragma unroll
    for (int p = 0; p < 7; ++p) issue1(st, p);
  };
  issue(0); issue(1);
  u32x2 rv[3];
  int ro[3];
#pragma unroll
  for (int t = 0; t < 3; ++t) {
    int n = 16 * (6 * ng + 3 * ch + t) + 4 * g;
    ro[t] = MODE == 2 ? ((n >> 3) * S + s) * 16 + (n & 4) * 2 : s * 1152 + n * 2;
  }
  STAMP(KID, 1);
  f32x4 acc[6] = {};
  bf16x8 wf[2][6];
  const char* wa = sm + (54 * ch + kh) * 1024 + l16;
  WAITV(7); BAR(); STAMP(KID, 2);
#pragma unroll
  for (int t = 0; t < 6; ++t) wf[0][t] = ldf(wa + ((t % 3) * 18 + 2 * (t / 3)) * 1024);
  issue(2);
#pragma unroll
  for (int st = 0; st < 5; ++st) {
    if (st < 4) {
      if (st < 3) WAITV(7); else WAITV(3);
      BAR();
    }
#pragma unroll
    for (int k = 0; k < (st < 4 ? 2 : 1); ++k) {
      PIN(xf[0][2 * st + k]); PIN(xf[1][2 * st + k]);
#pragma unroll
      for (int t = 0; t < 3; ++t) {
        acc[t] = mfma(wf[st & 1][3 * k + t], xf[0][2 * st + k], acc[t]); acc[3 + t] = mfma(wf[st & 1][3 * k + t], xf[1][2 * st + k], acc[3 + t]);
        SB();
        if (st < 3 || (st == 3 && k == 0)) wf[(st + 1) & 1][3 * k + t] = ldf(wa + (t * 18 + 4 * st + 4 + 2 * k) * 1024);
        SB();
        if (st < 2) { issue1(st + 3, 3 * k + t); if (k == 1 && t == 2) issue1(st + 3, 6); }
        if (st == 1 && k == 1 && t == 2) {
          const char* rp = ws + (MODE == 2 ? O_XH1 : O_X3);
          ALD8(rv[0], ro[0], rp); ALD8(rv[1], ro[1], rp); ALD8(rv[2], ro[2], rp);
        }
      }
    }
  }
  BAR(); STAMP(KID, 4);
#pragma unroll
  for (int t = 0; t < 3; ++t) *(f32x4*)(sm + (w * 3 + t) * 1024 + l16) = acc[3 + t];
  WAITV(0); BAR();
  char* op = MODE == 2 ? ws + O_XH2 : outp;
  float sq = 0.f;
#pragma unroll
  for (int t = 0; t < 3; ++t) {
    PIN(rv[t]);
    f32x4 v = acc[t] + *(const f32x4*)(sm + ((w ^ 4) * 3 + t) * 1024 + l16);
    v[0] += lo16(rv[t][0]); v[1] += hi16(rv[t][0]); v[2] += lo16(rv[t][1]); v[3] += hi16(rv[t][1]);
    *(u32x2*)(op + ro[t]) = pk4(v);
    if (MODE == 2) sq += v[0] * v[0] + v[1] * v[1] + v[2] * v[2] + ((t == 2 && ng == 5 && ch && g == 3) ? 0.f : v[3] * v[3]);
  }
  if (MODE == 2) {
    sq += __shfl_xor(sq, 16); sq += __shfl_xor(sq, 32);
    float* red = (float*)(sm + 32768);
    if (ch && g == 0) red[(2 * wr + kh) * 16 + m] = sq;
    BAR();
    if (!ch && g == 0) ((float*)(ws + O_SS2))[ng * S + s] = sq + red[(2 * wr + kh) * 16 + m];
  }
  FENCE(); STAMP(KID, 5);
}

__global__ void __launch_bounds__(512, 2) k_attn0(char* ws) { attn_body<0>(ws + O_XH1, ws); }
__global__ void __launch_bounds__(512, 2) k_out2(char* ws) { out_body<2>(ws, nullptr); }
__global__ void __launch_bounds__(512, 2) k_attn1(char* ws) { attn_body<1>(ws + O_XH3, ws); }
__global__ void __launch_bounds__(512, 2) k_out3(char* ws, char* outp) { out_body<3>(ws, outp); }

__global__ void __launch_bounds__(768, 6) k_pre(const char* x0, const char* wo, const char* wr, const char* wi, const char* wqkv, char* ws) {
  extern __shared__ __attribute__((aligned(16))) char sm[];
  const int b = blockIdx.x, tid = threadIdx.x;
  if (b < 192) {
    int x = b & 7, i = b >> 3;
    gemm_q<0>(sm, 4 * x + (i & 3), i >> 2, x0, ws + O_XH0, (float*)(ws + O_SS0));
  } else if (b < 246) {
    int u = b - 192;
    gemm_q<1>(sm, u % 9, u / 9, wo, ws + O_WOQ, nullptr);
  } else {
    STAMP(0, 0);
    int u = b - 246, tg = u >> 3, kb = 8 * (u & 7);
    u32x4 v[4], code;
    u32x2 wpf;
    {
      const char* pc; int len;
      asm volatile("s_getpc_b64 %0\n.Lpre_pc:\n\ts_mov_b32 %1, .Lpre_pc-k_fft1" : "=s"(pc), "=s"(len));
      int off = tg * 12288 + tid * 16;
      off = off + 16 <= len ? off : 0;
      const char* base = len > 0 ? pc - len : pc;
      ALD16(code, off, base, 0);
      int wq = u * 768 + tid; wq = (wq < 15552 ? wq : 0) * 128;
      ALD8(wpf, wq, wqkv);
    }
#pragma unroll
    for (int i = 0; i < 4; ++i) {
      int e = i * 768 + tid; e = e < 2312 ? e : 2311;
      int run = e / 289, o = e - 289 * run;
      v[i] = *(const u32x4*)((run < 4 ? wr : wi) + (kb + 64 * (4 * tg + (run & 3))) * 578 + o * 16);
    }
    {
      const int id = u * 768 + tid;
      *(u32x4*)(ws + O_G + id * 16) = (u32x4){0u, 0u, 0u, 0u};
      if (id < 4608) *(u32x4*)(ws + O_WF + ((7 * (id / 288) + 6) * 18 + (id % 288 >> 4)) * 1024 + (12 + (id & 3) + 16 * ((id >> 2) & 3)) * 16) = (u32x4){0u, 0u, 0u, 0u};
    }
#pragma unroll
    for (int i = 0; i < 4; ++i) { int e = i * 768 + tid; e = e < 2312 ? e : 2311; *(u32x4*)(sm + e * 16) = v[i]; }
    __syncthreads();
    STAMP(0, 2);
#pragma unroll
    for (int i = 0; i < 3; ++i) {
      int e = i * 768 + tid, mm = e & 7, c = e >> 3;
      const char* p = sm + mm * 578 + 2 * (c + 1);
      u32x4 o;
#pragma unroll
      for (int r = 0; r < 4; ++r) o[r] = (unsigned)*(const u16*)(p + r * 4624) | ((unsigned)*(const u16*)(p + (4 + r) * 4624) << 16);
      *(u32x4*)(ws + O_WT + ((c * 4 + (kb >> 4)) * 2 + (tg >> 2)) * 1024 + (16 * (tg & 3) + (kb & 8) + mm) * 16) = o;
    }
    WAITV(0); PIN(code); PIN(wpf);
    FENCE(); STAMP(0, 5);
  }
}

extern "C" void kernel_launch(void* const* d_in, const int* in_sizes, int n_in, void* d_out, int out_size, void* d_ws,
                              size_t ws_size, hipStream_t stream) {
  const char *x0 = (const char*)d_in[0], *wr = (const char*)d_in[1], *wi = (const char*)d_in[2];
  const char *wqkv = (const char*)d_in[4], *wo = (const char*)d_in[5];
  char* ws = (char*)d_ws;
  (void)hipFuncSetAttribute((const void*)k_pre, hipFuncAttributeMaxDynamicSharedMemorySize, 73728);
  (void)hipFuncSetAttribute((const void*)k_fft1, hipFuncAttributeMaxDynamicSharedMemorySize, 73728);
  (void)hipFuncSetAttribute((const void*)k_attn0, hipFuncAttributeMaxDynamicSharedMemorySize, 163328);
  (void)hipFuncSetAttribute((const void*)k_attn1, hipFuncAttributeMaxDynamicSharedMemorySize, 163328);
  (void)hipFuncSetAttribute((const void*)k_out2, hipFuncAttributeMaxDynamicSharedMemorySize, 110592);
  (void)hipFuncSetAttribute((const void*)k_out3, hipFuncAttributeMaxDynamicSharedMemorySize, 110592);
  k_pre<<<310, 768, 73728, stream>>>(x0, wo, wr, wi, wqkv, ws);
  k_fft1<<<252, 512, 73728, stream>>>(wqkv, ws);
  k_attn0<<<256, 512, 163328, stream>>>(ws);
  k_out2<<<192, 512, 110592, stream>>>(ws);
  k_fft2<<<252, 512, 40960, stream>>>(wo, ws);
  k_attn1<<<256, 512, 163328, stream>>>(ws);
  k_out3<<<192, 512, 110592, stream>>>(ws, (char*)d_out);
}
```

```cpp
#include <hip/hip_runtime.h>
#include <hip/hip_bf16.h>

typedef __bf16 bf16x8 __attribute__((ext_vector_type(8)));
typedef __bf16 bf16x2 __attribute__((ext_vector_type(2)));
typedef short s16x4 __attribute__((ext_vector_type(4)));
typedef float f32x4 __attribute__((ext_vector_type(4)));
typedef float f32x2 __attribute__((ext_vector_type(2)));
typedef unsigned u32x2 __attribute__((ext_vector_type(2)));
typedef unsigned u32x4 __attribute__((ext_vector_type(4)));
typedef unsigned short u16;
#define LDSP __attribute__((address_space(3)))
#define GLBP __attribute__((address_space(1)))
#define DI __device__ __forceinline__
#define FENCE() asm volatile("" ::: "memory")
#define WAITV(n) asm volatile("s_waitcnt vmcnt(" #n ")" ::: "memory")
#define SB() __builtin_amdgcn_sched_barrier(0)
#define BAR() do { SB(); asm volatile("s_waitcnt lgkmcnt(0)" ::: "memory"); __builtin_amdgcn_s_barrier(); FENCE(); SB(); } while (0)

#define STAMP(k, i) do {} while (0)
constexpr int S = 2048, D = 576;
constexpr bool ALIAS = false;
constexpr size_t XB = (size_t)S * D * 2;
constexpr size_t O_XH0 = 0, O_XH1 = XB, O_XH2 = ALIAS ? 0 : 2 * XB, O_XH3 = ALIAS ? XB : 3 * XB, O_OB = (ALIAS ? 2 : 4) * XB;
constexpr size_t O_WT = (ALIAS ? 3 : 6) * XB, O_X3 = ALIAS ? O_WT : 5 * XB;
constexpr size_t O_WOQ = ALIAS ? 4 * XB : 7 * XB + 663552, O_WOF = O_WOQ + 663552, O_WF = O_WOF + 663552;
constexpr size_t O_QTF = O_WF + 2064384, O_SS0 = O_QTF + 884736, O_SS2 = O_SS0 + 8192, O_G = O_SS2 + 6 * 8192;
constexpr int GP = 3072;
constexpr size_t GL = (size_t)256 * GP;

DI f32x4 mfma(bf16x8 a, bf16x8 b, f32x4 c) { return __builtin_amdgcn_mfma_f32_16x16x32_bf16(a, b, c, 0, 0, 0); }
DI unsigned pk2(float a, float b) { f32x2 f = {a, b}; return __builtin_bit_cast(unsigned, __builtin_convertvector(f, bf16x2)); }
DI bf16x8 pk8(f32x4 a, f32x4 b) { u32x4 u = {pk2(a[0], a[1]), pk2(a[2], a[3]), pk2(b[0], b[1]), pk2(b[2], b[3])}; return __builtin_bit_cast(bf16x8, u); }
DI float lo16(unsigned u) { return __builtin_bit_cast(float, u << 16); }
DI float hi16(unsigned u) { return __builtin_bit_cast(float, u & 0xffff0000u); }
DI float cosr(float rev) { return __builtin_amdgcn_cosf(__builtin_amdgcn_fractf(rev)); }
DI float sinr(float rev) { return __builtin_amdgcn_sinf(__builtin_amdgcn_fractf(rev)); }
DI void glds(const char* g, char* l) { __builtin_amdgcn_global_load_lds((const GLBP void*)g, (LDSP void*)l, 16, 0, 0); }
DI bf16x8 ldf(const char* p) { return *(const bf16x8*)p; }
typedef __amdgpu_buffer_rsrc_t rsrc_t;
DI rsrc_t mkrs(const void* p, unsigned bytes) { return __builtin_amdgcn_make_buffer_rsrc((void*)p, 0, bytes, 0x00020000); }
DI void bglds(rsrc_t r, char* l, int voff, int soff) { __builtin_amdgcn_raw_ptr_buffer_load_lds(r, (LDSP void*)l, 16, voff, soff, 0, 0); }
#define ALD16(dst, voff, sbase, imm) asm volatile("global_load_dwordx4 %0, %1, %2 offset:" #imm : "=v"(dst) : "v"(voff), "s"(sbase))
#define ALD8(dst, voff, sbase) asm volatile("global_load_dwordx2 %0, %1, %2" : "=v"(dst) : "v"(voff), "s"(sbase))
#define PIN(x) asm volatile("" : "+v"(x))
DI bf16x8 trf(const char* p, int d) {
  s16x4 lo = __builtin_amdgcn_ds_read_tr16_b64_v4i16((LDSP s16x4*)p), hi = __builtin_amdgcn_ds_read_tr16_b64_v4i16((LDSP s16x4*)(p + d));
  return __builtin_bit_cast(bf16x8, __builtin_shufflevector(lo, hi, 0, 1, 2, 3, 4, 5, 6, 7));
}
struct St4 { char* p; DI void operator=(u32x4 v) const { asm volatile("global_store_dwordx4 %0, %1, off sc1\n\ts_nop 1" :: "v"(p), "v"(v) : "memory"); } };
struct St2 { char* p; DI void operator=(u32x2 v) const { *(u32x2*)p = v; } };
DI u32x2 pk4(f32x4 v) { return (u32x2){pk2(v[0], v[1]), pk2(v[2], v[3])}; }
constexpr u32x4 ONES = {0x3f803f80u, 0x3f803f80u, 0x3f803f80u, 0x3f803f80u};
constexpr f32x4 Z4 = {0.f, 0.f, 0.f, 0.f};
DI float qent(int d, int j) {
  int f = (j >> 1) + 1; float ph = (j & 1) ? 0.25f : 0.f, sc = 0.05892556509887896f;
  if (j >= 574) { sc = 0.041666666666666664f; ph = 0.f; f = (j == 575) ? 0 : 288; }
  return sc * cosr((float)(f * d) * (1.f / 576.f) + ph);
}
DI int operm(int k) {
  int hp = k / 36, c = k - 36 * hp, p = c >> 2, i = c & 3;
  return p < 8 ? 18 * (2 * hp + (p >> 2)) + 4 * (p & 3) + i : 18 * (2 * hp + (i >> 1)) + 16 + (i & 1);
}

DI void qgen(bf16x8* qf, int j, int d0, int dd) {
  int f = (j >> 1) + 1; float ph = (j & 1) ? 0.25f : 0.f, sc = 0.05892556509887896f;
  if (j >= 574) { sc = 0.041666666666666664f; ph = 0.f; f = (j == 575) ? 0 : 288; }
  const float st = (float)f * (1.f / 576.f), c2 = 2.f * __builtin_amdgcn_cosf(st), p0 = st * (float)d0 + ph;
#pragma unroll
  for (int i = 0; i < 9; ++i) {
    float p = __builtin_amdgcn_fractf(st * (float)(dd * i) + p0), v[8];
    v[0] = sc * __builtin_amdgcn_cosf(p); v[1] = sc * __builtin_amdgcn_cosf(p + st);
#pragma unroll
    for (int e = 2; e < 8; ++e) v[e] = c2 * v[e - 1] - v[e - 2];
    qf[i] = __builtin_bit_cast(bf16x8, (u32x4){pk2(v[0], v[1]), pk2(v[2], v[3]), pk2(v[4], v[5]), pk2(v[6], v[7])});
    PIN(qf[i]);
  }
}
template <int MODE>
DI void gemm_q(char* sm, int rb, int ng, const char* xs, char* out, float* ssq) {
  const int tid = threadIdx.x, w = __builtin_amdgcn_readfirstlane(tid >> 6), l = tid & 63, g = l >> 4, m = l & 15;
  const int kh = w >= 6, nt = w - 6 * kh, jt = 6 * ng + nt;
  STAMP(0, 0);
  {
    const rsrc_t xrs = mkrs(xs, MODE == 0 ? (unsigned)XB : 663552u);
    int voff[2];
#pragma unroll
    for (int h = 0; h < 2; ++h) {
      int r = 64 * rb + 8 * ((w + 4 * h) & 7) + (l >> 3);
      voff[h] = (MODE == 1 ? operm(r) : r) * 1152 + (((l & 7) ^ (l >> 3)) << 4);
    }
#pragma unroll
    for (int i = 0; i < 6; ++i) { int q = w + 12 * i; bglds(xrs, sm + q * 1024, voff[i & 1], (q >> 3) * 128); }
  }
  SB(); STAMP(0, 1);
  bf16x8 qf[9];
  qgen(qf, 16 * jt + m, 8 * g + 32 * kh, 64);
  WAITV(0); BAR(); STAMP(0, 2);
  f32x4 acc[4] = {}, gr = Z4, su = Z4;
  const bool stats = MODE == 0 && ng == 0 && nt < 4;
  const int x2 = 2 * kh;
  const char* xb = sm + (m >> 3) * 1024 + (m & 7) * 128 + (((4 * kh + g) ^ (m & 7)) << 4);
#pragma unroll
  for (int i = 0; i < 9; ++i) {
#pragma unroll
    for (int a = 0; a < 4; ++a) {
      bf16x8 xf = ldf(xb + i * 8192 + (a ^ x2) * 2048);
      acc[a] = MODE == 1 ? mfma(xf, qf[i], acc[a]) : mfma(qf[i], xf, acc[a]);
    }
    if (stats) { bf16x8 xf = ldf(xb + i * 8192 + nt * 2048); gr = mfma(xf, xf, gr); su = mfma(__builtin_bit_cast(bf16x8, ONES), xf, su); }
  }
  BAR(); STAMP(0, 4);
  *(f32x4*)(sm + w * 4096 + l * 16) = acc[2]; *(f32x4*)(sm + w * 4096 + 1024 + l * 16) = acc[3];
  if (stats && kh) { *(f32x4*)(sm + w * 4096 + 2048 + l * 16) = gr; *(f32x4*)(sm + w * 4096 + 3072 + l * 16) = su; }
  BAR();
  const char* pp = sm + (kh ? w - 6 : w + 6) * 4096 + l * 16;
#pragma unroll
  for (int a = 0; a < 2; ++a) {
    f32x4 v = acc[a] + *(const f32x4*)(pp + a * 1024);
    const int rt = 4 * rb + x2 + a, n = 16 * jt + 4 * g;
    if (MODE == 1) St2{out + (jt * 18 + (rt >> 1)) * 1024 + (m + 16 * (2 * (rt & 1) + (g >> 1))) * 16 + (g & 1) * 8} = pk4(v);
    else St2{out + ((n >> 3) * S + 16 * rt + m) * 16 + (n & 4) * 2} = pk4(v);
  }
  if (stats && !kh) {
    gr += *(const f32x4*)(pp + 2048); su += *(const f32x4*)(pp + 3072);
    float dsel = (m & 2) ? ((m & 1) ? gr[3] : gr[2]) : ((m & 1) ? gr[1] : gr[0]);
    if ((m >> 2) == g) ssq[64 * rb + 16 * nt + m] = fmaxf(dsel - su[0] * su[0] * (1.f / 576.f), 0.f);
  }
  FENCE(); STAMP(0, 5);
}

template <int NP>
DI void fft_cols(char* sm, int b, const char* xin, char* xout, const float* ssq, const char* wt) {
  const int tid = threadIdx.x, w8 = __builtin_amdgcn_readfirstlane(tid >> 6), l = tid & 63, g = l >> 4, m = l & 15;
  const int w = w8 & 3, hi = w8 >> 2;
  const int kb = (b & 7) + 8 * (b >> 4), half = (b >> 3) & 1;
  const bool last = kb == 71 && half == 1;
  constexpr int KID = NP == 1 ? 1 : 4;
  STAMP(KID, 0);
  f32x4 ps[NP];
#pragma unroll
  for (int p = 0; p < NP; ++p) ps[p] = *(const f32x4*)(ssq + p * S + tid * 4);
  u32x2 xv[4];
  const int xo = (kb * S + 32 * (16 * w + 4 * g) + 16 * hi + m) * 16 + 8 * half;
#pragma unroll
  for (int r = 0; r < 4; ++r) xv[r] = *(const u32x2*)(xin + xo + r * 512);
  u32x4 wv[2];
#pragma unroll
  for (int t = 0; t < 2; ++t) wv[t] = *(const u32x4*)(wt + (((4 * kb + 2 * half + hi) * 4 + w) * 2 + t) * 1024 + l * 16);
  FENCE(); STAMP(KID, 1);
  SB();
  bf16x8 c64[2], s64[2], n64[2], c32[2], s32[2], n32[2];
  f32x4 t1c[2], t1s[2], t2c[2], t2s[2];
  {
    const float k1 = (float)(16 * w + m), g4 = (float)(4 * g);
    const unsigned sg = (m & 1) ? 0x80008000u : 0u;
    float c[8], sn[8];
    const float a = k1 * (1.f / 64.f), a2 = 2.f * __builtin_amdgcn_cosf(a), p = a * (g4 + g4);
    c[0] = __builtin_amdgcn_cosf(p); c[1] = __builtin_amdgcn_cosf(p + a); sn[0] = __builtin_amdgcn_sinf(p); sn[1] = __builtin_amdgcn_sinf(p + a);
#pragma unroll
    for (int j = 2; j < 8; ++j) { c[j] = a2 * c[j - 1] - c[j - 2]; sn[j] = a2 * sn[j - 1] - sn[j - 2]; }
    u32x4 uc = {pk2(c[0], c[1]), pk2(c[2], c[3]), pk2(c[4], c[5]), pk2(c[6], c[7])}, us = {pk2(sn[0], sn[1]), pk2(sn[2], sn[3]), pk2(sn[4], sn[5]), pk2(sn[6], sn[7])};
    c64[0] = __builtin_bit_cast(bf16x8, uc); c64[1] = __builtin_bit_cast(bf16x8, uc ^ sg);
    s64[0] = __builtin_bit_cast(bf16x8, us); s64[1] = __builtin_bit_cast(bf16x8, us ^ sg);
    n64[0] = __builtin_bit_cast(bf16x8, us ^ 0x80008000u); n64[1] = __builtin_bit_cast(bf16x8, us ^ (sg ^ 0x80008000u));
    const float e = (float)m * (1.f / 32.f);
#pragma unroll
    for (int j = 0; j < 4; ++j) { float q = e * (g4 + (float)j); c[j] = __builtin_amdgcn_cosf(q); sn[j] = __builtin_amdgcn_sinf(q); }
    unsigned c0 = pk2(c[0], c[1]), c1 = pk2(c[2], c[3]), s0 = pk2(sn[0], sn[1]), s1 = pk2(sn[2], sn[3]);
    uc = (u32x4){c0, c1, c0 ^ sg, c1 ^ sg}; us = (u32x4){s0, s1, s0 ^ sg, s1 ^ sg};
    c32[0] = __builtin_bit_cast(bf16x8, uc); c32[1] = __builtin_bit_cast(bf16x8, uc ^ 0x80000000u);
    s32[0] = __builtin_bit_cast(bf16x8, us); s32[1] = __builtin_bit_cast(bf16x8, us ^ 0x80000000u);
    n32[0] = __builtin_bit_cast(bf16x8, us ^ 0x80008000u); n32[1] = __builtin_bit_cast(bf16x8, us ^ 0x00008000u);
    const float b1 = k1 * (1.f / 2048.f), b12 = 2.f * __builtin_amdgcn_cosf(b1);
#pragma unroll
    for (int h = 0; h < 2; ++h) {
      const float p1 = b1 * (g4 + (float)(16 * h)), b2 = (float)(16 * h + m) * (1.f / 2048.f), b22 = 2.f * __builtin_amdgcn_cosf(b2), p2 = b2 * (g4 + (float)(16 * w));
      t1c[h][0] = __builtin_amdgcn_cosf(p1); t1c[h][1] = __builtin_amdgcn_cosf(p1 + b1); t1s[h][0] = __builtin_amdgcn_sinf(p1); t1s[h][1] = __builtin_amdgcn_sinf(p1 + b1);
      t2c[h][0] = __builtin_amdgcn_cosf(p2); t2c[h][1] = __builtin_amdgcn_cosf(p2 + b2); t2s[h][0] = __builtin_amdgcn_sinf(p2); t2s[h][1] = __builtin_amdgcn_sinf(p2 + b2);
#pragma unroll
      for (int r = 2; r < 4; ++r) {
        t1c[h][r] = b12 * t1c[h][r - 1] - t1c[h][r - 2]; t1s[h][r] = b12 * t1s[h][r - 1] - t1s[h][r - 2];
        t2c[h][r] = b22 * t2c[h][r - 1] - t2c[h][r - 2]; t2s[h][r] = b22 * t2s[h][r - 1] - t2s[h][r - 2];
      }
      PIN(c64[h]); PIN(s64[h]); PIN(n64[h]); PIN(c32[h]); PIN(s32[h]); PIN(n32[h]); PIN(t1c[h]); PIN(t1s[h]); PIN(t2c[h]); PIN(t2s[h]);
    }
  }
  SB();
  FENCE(); STAMP(KID, 2);
  float* rsl = (float*)(sm + 32768);
  {
    f32x4 a = ps[0];
#pragma unroll
    for (int p = 1; p < NP; ++p) a += ps[p];
#pragma unroll
    for (int i = 0; i < 4; ++i) a[i] = __builtin_amdgcn_rsqf(a[i] * (1.f / 576.f) + 1e-5f) * (1.f / 2048.f);
    *(f32x4*)(rsl + tid * 4) = a;
  }
  BAR();
  const int wo = m * 128 + (((2 * w + (g >> 1)) ^ (m & 7)) << 4) + (g & 1) * 8;
  {
    f32x4 u[4];
#pragma unroll
    for (int r = 0; r < 4; ++r) {
      float rs = rsl[32 * (16 * w + 4 * g + r) + 16 * hi + m];
      u[0][r] = lo16(xv[r][0]) * rs; u[1][r] = hi16(xv[r][0]) * rs; u[2][r] = lo16(xv[r][1]) * rs;
      u[3][r] = last ? 0.f : hi16(xv[r][1]) * rs;
    }
#pragma unroll
    for (int p = 0; p < 4; ++p) *(u32x2*)(sm + p * 4096 + hi * 2048 + wo) = pk4(u[p]);
  }
  BAR(); STAMP(KID, 3);
  const int ro0 = m * 128 + ((g ^ (m & 7)) << 4), ro1 = ro0 ^ 64;
  {
    f32x4 re[2], im[2];
#pragma unroll
    for (int t = 0; t < 2; ++t) {
      const char* p = sm + hi * 8192 + t * 2048;
      bf16x8 a0 = ldf(p + ro0), a1 = ldf(p + ro1), b0 = ldf(p + 4096 + ro0), b1 = ldf(p + 4096 + ro1);
      f32x4 x = mfma(a0, c64[0], Z4), y = mfma(b0, c64[0], Z4);
      x = mfma(a1, c64[1], x); y = mfma(b1, c64[1], y);
      x = mfma(b0, s64[0], x); y = mfma(a0, n64[0], y);
      x = mfma(b1, s64[1], x); y = mfma(a1, n64[1], y);
#pragma unroll
      for (int r = 0; r < 4; ++r) { re[t][r] = x[r] * t1c[t][r] + y[r] * t1s[t][r]; im[t][r] = y[r] * t1c[t][r] - x[r] * t1s[t][r]; }
    }
    bf16x8 fr = pk8(re[0], re[1]), fi = pk8(im[0], im[1]);
#pragma unroll
    for (int t = 0; t < 2; ++t) {
      f32x4 x = mfma(c32[t], fr, Z4), y = mfma(c32[t], fi, Z4);
      x = mfma(s32[t], fi, x); y = mfma(n32[t], fr, y);
#pragma unroll
      for (int r = 0; r < 4; ++r) {
        float a = lo16(wv[t][r]), bb = hi16(wv[t][r]);
        re[t][r] = x[r] * a - y[r] * bb; im[t][r] = x[r] * bb + y[r] * a;
      }
    }
    fr = pk8(re[0], re[1]); fi = pk8(im[0], im[1]);
#pragma unroll
    for (int t = 0; t < 2; ++t) {
      f32x4 x = mfma(fr, c32[t], Z4), y = mfma(fi, c32[t], Z4);
      x = mfma(fi, n32[t], x); y = mfma(fr, s32[t], y);
      f32x4 vr, vi;
#pragma unroll
      for (int r = 0; r < 4; ++r) { vr[r] = x[r] * t2c[t][r] - y[r] * t2s[t][r]; vi[r] = y[r] * t2c[t][r] + x[r] * t2s[t][r]; }
      char* p = sm + 16384 + hi * 8192 + t * 2048 + wo;
      *(u32x2*)p = pk4(vr); *(u32x2*)(p + 4096) = pk4(vi);
    }
  }
  BAR(); STAMP(KID, 4);
  {
    f32x4 yr[2], yi[2];
#pragma unroll
    for (int c = 0; c < 2; ++c) {
      const char* p = sm + 16384 + c * 8192 + hi * 2048;
      bf16x8 a0 = ldf(p + ro0), a1 = ldf(p + ro1), b0 = ldf(p + 4096 + ro0), b1 = ldf(p + 4096 + ro1);
      f32x4 x = mfma(c64[0], a0, Z4), y = mfma(c64[0], b0, Z4);
      x = mfma(c64[1], a1, x); y = mfma(c64[1], b1, y);
      x = mfma(n64[0], b0, x); y = mfma(s64[0], a0, y);
      x = mfma(n64[1], b1, x); y = mfma(s64[1], a1, y);
      yr[c] = x; yi[c] = y;
    }
#pragma unroll
    for (int r = 0; r < 4; ++r) {
      u32x2 o = {pk2(lo16(xv[r][0]) + yr[0][r], hi16(xv[r][0]) + yi[0][r]),
                 pk2(lo16(xv[r][1]) + yr[1][r], hi16(xv[r][1]) + (last ? 0.f : yi[1][r]))};
      St2{xout + xo + r * 512} = o;
    }
  }
  FENCE(); STAMP(KID, 5);
}

DI void wqkv_rot(char* sm, int u, const char* wqkv, char* wf) {
  const int tid = threadIdx.x, w = __builtin_amdgcn_readfirstlane(tid >> 6), l = tid & 63, g = l >> 4, m = l & 15;
  const int jg = u & 3, ng = u >> 2, kh = w >> 2, wc = w & 3, jt = 9 * jg + 2 * wc + kh, j8 = 9 * jg + 8;
  STAMP(1, 0);
  const rsrc_t rs = mkrs(wqkv, 1990656u);
  const int voff = (l >> 3) * 3456 + (((((l >> 1) & 3) ^ (((l >> 4) & 1) | ((w & 1) << 1))) << 5) | ((l & 1) << 4));
  const int l16 = l * 16;
#pragma unroll
  for (int i = 0; i < 9; ++i) bglds(rs, sm + (w + 8 * i) * 1024, voff, (w + 8 * i) * 27648 + ng * 128);
  SB();
  bf16x8 af[3][9];
  qgen(af[0], 16 * jt + m, 288 * kh + 8 * g, 32);
  qgen(af[1], 16 * (jt + 1 - 2 * kh) + m, 288 * kh + 8 * g, 32);
  qgen(af[2], 16 * j8 + m, 288 * kh + 8 * g, 32);
  int ob[4];
#pragma unroll
  for (int t = 0; t < 4; ++t) {
    int n = 64 * ng + 16 * (t ^ wc) + m, seg = (n >= 576) + (n >= 1152), c = n - 576 * seg, hp = c / 36;
    int cc = c - 36 * hp + (seg == 0 ? 72 : seg == 1 ? 0 : 36);
    ob[t] = (7 * hp + (cc >> 4)) * 18432 + (cc & 15) * 16;
  }
  STAMP(1, 2);
  WAITV(0); BAR(); STAMP(1, 3);
  f32x4 acc[2][4] = {}, a8 = Z4;
  const int rbase = 9 * kh * 4096 + (8 * g + (m >> 2)) * 128 + 8 * (m & 3), fx = (((m >> 3) & 1) | ((g & 1) << 1)) ^ wc;
#pragma unroll
  for (int i = 0; i < 9; ++i) {
#pragma unroll
    for (int t = 0; t < 4; ++t) {
      bf16x8 bfr = trf(sm + i * 4096 + rbase + ((t ^ fx) << 5), 512);
      acc[0][t] = mfma(af[0][i], bfr, acc[0][t]); acc[1][t] = mfma(af[1][i], bfr, acc[1][t]);
      if (t == 0) a8 = mfma(af[2][i], bfr, a8);
    }
  }
  BAR(); STAMP(1, 4);
#pragma unroll
  for (int t = 0; t < 4; ++t) *(f32x4*)(sm + (w * 5 + t) * 1024 + l16) = acc[1][t];
  *(f32x4*)(sm + (w * 5 + 4) * 1024 + l16) = a8;
  BAR();
  const int jo = (jt >> 1) * 1024 + (2 * (jt & 1) + (g >> 1)) * 256 + (g & 1) * 8;
#pragma unroll
  for (int t = 0; t < 4; ++t) St2{wf + ob[t] + jo} = pk4(acc[0][t] + *(const f32x4*)(sm + ((w ^ 4) * 5 + t) * 1024 + l16));
  if (!kh) {
    f32x4 o = a8 + *(const f32x4*)(sm + ((w ^ 4) * 5 + 4) * 1024 + l16);
    if (j8 == 35 && g == 3) o[3] = 0.f;
    St2{wf + ob[0] + (j8 >> 1) * 1024 + (2 * (j8 & 1) + (g >> 1)) * 256 + (g & 1) * 8} = pk4(o);
  }
  FENCE(); STAMP(1, 5);
}

extern "C" __global__ void __launch_bounds__(512, 2) k_fft1(const char* wqkv, char* ws) {
  extern __shared__ __attribute__((aligned(16))) char sm[];
  const int b = blockIdx.x;
  if (b < 144) fft_cols<1>(sm, b, ws + O_XH0, ws + O_XH1, (const float*)(ws + O_SS0), ws + O_WT);
  else wqkv_rot(sm, b - 144, wqkv, ws + O_WF);
}
__global__ void __launch_bounds__(512, 2) k_fft2(const char* wo, char* ws) {
  extern __shared__ __attribute__((aligned(16))) char sm[];
  const int b = blockIdx.x;
  if (b < 144) fft_cols<6>(sm, b, ws + O_XH2, ws + O_XH3, (const float*)(ws + O_SS2), ws + O_WT);
  else {
    const int e = (b - 144) * 512 + threadIdx.x, l = e & 63, pc = e >> 6, nt = pc / 18, ks = pc - 18 * nt;
    unsigned h[8];
    if (e < 41472) {
      const int r0 = operm(32 * ks + 8 * (l >> 4)), r1 = operm(32 * ks + 8 * (l >> 4) + 4), c0 = (32 * ks + 8 * (l >> 4)) % 36;
#pragma unroll
      for (int j = 0; j < 8; ++j) {
        int r = (j < 4 ? r0 : r1) + (c0 == (j < 4 ? 32 : 28) ? 18 * ((j >> 1) & 1) + (j & 1) : (j & 3));
        h[j] = *(const u16*)(wo + r * 1152 + (16 * nt + (l & 15)) * 2);
      }
    }
    if (e < (int)(GL / 16)) St4{ws + O_G + GL + e * 16} = (u32x4){0u, 0u, 0u, 0u};
    const int hp = nt / 3, c = 16 * (nt - 3 * hp) + (l & 15), d = 36 * hp + (c < 36 ? c : 0), j0 = 32 * ks + 8 * (l >> 4);
    St4{ws + O_QTF + e * 16} = (u32x4){pk2(qent(d, j0), qent(d, j0 + 1)), pk2(qent(d, j0 + 2), qent(d, j0 + 3)),
                                             pk2(qent(d, j0 + 4), qent(d, j0 + 5)), pk2(qent(d, j0 + 6), qent(d, j0 + 7))};
    if (e < 41472) St4{ws + O_WOF + e * 16} = (u32x4){h[0] | (h[1] << 16), h[2] | (h[3] << 16), h[4] | (h[5] << 16), h[6] | (h[7] << 16)};
  }
}

constexpr int A_QT = 103936, A_KV = A_QT + 55296;
constexpr int A_PK = 65536, A_V = A_PK + 13056, A_Q = A_V + 13056;
template <int LAST>
DI void attn_body(const char* xin, char* ws) {
  extern __shared__ __attribute__((aligned(16))) char sm[];
  const int tid = threadIdx.x, w = __builtin_amdgcn_readfirstlane(tid >> 6), l = tid & 63, g = l >> 4, m = l & 15;
  const int b = blockIdx.x, rb = 4 * ((b & 7) >> 1) + ((b >> 3) & 3), hp = 8 * (b & 1) + (b >> 5);
  const int wr = w & 3, kh = w >> 2, s0 = 128 * rb + 32 * wr;
  constexpr int KID = LAST ? 5 : 2;
  STAMP(KID, 0);
  const rsrc_t wrs = mkrs(ws + O_WF + hp * 129024, 129024);
  const int l16 = l * 16, xo0 = (g * S + s0 + 16 * kh + m) * 16, xo1 = xo0 ^ 256;
  bf16x8 xf[2][9];
  auto issue1 = [&](int j, int p) {
    if (p < 2) {
      int q = w + 8 * p; q = q < 14 ? q : q - 8;
      int pc = (q >> 1) * 18 + 2 * j + (q & 1);
      bglds(wrs, sm + pc * 1024, l16, pc * 1024);
    } else {
      const char* xb = xin + (2 * j + kh) * (4 * S * 16);
      if (p == 2) ALD16(xf[0][j], xo0, xb, 0); else ALD16(xf[1][j], xo1, xb, 0);
    }
    FENCE(); SB();
  };
  auto issue = [&](int j) { issue1(j, 0); issue1(j, 1); issue1(j, 2); issue1(j, 3); };
#pragma unroll
  for (int j = 0; j < 3; ++j) issue(j);
  if (tid < 256) *(u32x4*)(sm + A_KV + tid * 16) = (u32x4){0u, 0u, 0u, 0u};
  const int pa = w < 6 ? w / 3 : 2, pb = w < 6 ? w - 3 * pa : w - 5;
  const int dq = 4 * pa + g, ec = 16 * pb + m - 4, eh = ec >= 18;
  const bool isz = ec == 36, valid = isz ? dq < 9 : (ec >= 0 && ec < 36 && dq >= 4 * eh && dq <= 4 * eh + 4);
  const int slot = (isz ? 180 + dq : 36 * (dq - 4 * eh) + ec) * 16;
  STAMP(KID, 1);
  f32x4 acc[2][7] = {};
  float sq[2] = {0.f, 0.f};
  bf16x8 wf[2][7];
  WAITV(8); BAR(); STAMP(KID, 2);
#pragma unroll
  for (int t = 0; t < 7; ++t) wf[0][t] = ldf(sm + (t * 18 + kh) * 1024 + l16);
  issue(3);
#pragma unroll
  for (int i = 0; i < 9; ++i) {
    if (i < 8) {
      if (i <= 5) WAITV(8); else if (i == 6) WAITV(4); else WAITV(0);
      BAR();
    }
    PIN(xf[0][i]); PIN(xf[1][i]);
#pragma unroll
    for (int t = 0; t < 7; ++t) {
      acc[0][t] = mfma(xf[0][i], wf[i & 1][t], acc[0][t]); acc[1][t] = mfma(xf[1][i], wf[i & 1][t], acc[1][t]);
      if (t < 4) {
#pragma unroll
        for (int a = 0; a < 2; ++a) {
          unsigned u = __builtin_bit_cast(u32x4, xf[a][i])[t];
          if (i == 8 && t == 3) u = (g == 3 && kh) ? (u & 0xffffu) : u;
          bf16x2 h = __builtin_bit_cast(bf16x2, u);
          sq[a] = __builtin_amdgcn_fdot2_f32_bf16(h, h, sq[a], false);
        }
      }
      SB();
      if (i < 8) wf[(i + 1) & 1][t] = ldf(sm + (t * 18 + 2 * i + 2 + kh) * 1024 + l16);
      SB();
      if (!(t & 1) && i + 4 < 9) issue1(i + 4, t >> 1);
    }
  }
  BAR(); STAMP(KID, 3);
  if (LAST) {
    const rsrc_t qrs = mkrs(ws + O_QTF + hp * 55296, 55296);
#pragma unroll
    for (int i = 0; i < 7; ++i) { int q = w + 8 * i; q = q < 54 ? q : q - 8; bglds(qrs, sm + A_QT + q * 1024, l16, q * 1024); }
  }
  SB();
#pragma unroll
  for (int t = 0; t < 7; ++t) *(f32x4*)(sm + (w * 8 + t) * 1024 + l16) = acc[1][t];
  *(float*)(sm + (w * 8 + 7) * 1024 + l * 4) = sq[1];
  BAR();
  float rq = sq[0] + *(const float*)(sm + ((w ^ 4) * 8 + 7) * 1024 + l * 4);
  f32x4 v[7];
#pragma unroll
  for (int t = 0; t < 7; ++t) v[t] = acc[0][t] + *(const f32x4*)(sm + ((w ^ 4) * 8 + t) * 1024 + l16);
  rq += __shfl_xor(rq, 16); rq += __shfl_xor(rq, 32);
  rq = __builtin_amdgcn_rsqf(rq * (1.f / 576.f) + 1e-5f);
  {
    f32x4 rs, rl;
#pragma unroll
    for (int r = 0; r < 4; ++r) { rs[r] = __shfl(rq, 4 * g + r); rl[r] = rs[r] * 1.4426950408889634f; }
    const int so = (32 * wr + 16 * kh + 4 * g) * 2 + m * 272;
#pragma unroll
    for (int t = 0; t < 7; ++t) {
      if (t != 3) {
        f32x4 p;
        const f32x4 z = v[t] * rl, y = __builtin_elementwise_fma(v[t], rs, (f32x4){1e-6f, 1e-6f, 1e-6f, 1e-6f});
        f32x4 e;
#pragma unroll
        for (int r = 0; r < 4; ++r) { e[r] = __builtin_amdgcn_fmed3f(__builtin_amdgcn_exp2f(z[r]), 0.f, 1.f); p[r] = fmaxf(y[r], 1e-6f); }
        p += e;
        if (t < 3) *(u32x2*)(sm + A_PK + t * 4352 + so) = pk4(p);
        else *(u32x2*)(sm + A_Q + w * 1536 + (16 * (t - 4) + m) * 32 + g * 8) = pk4(p);
      }
      if (t >= 2 && t <= 4) {
        u32x2 u = pk4(v[t] * rs);
        if (t == 4 && m == 8) u = (u32x2){0x3f803f80u, 0x3f803f80u};
        *(u32x2*)(sm + A_V + (t - 2) * 4352 + so) = u;
      }
    }
  }
  BAR(); STAMP(KID, 4);
  const rsrc_t grs = mkrs(ws + O_G + (LAST ? GL : 0) + hp * 16 * GP, 16 * GP);
  {
    f32x4 c = Z4;
#pragma unroll
    for (int k = 0; k < 4; ++k)
      c = mfma(ldf(sm + A_PK + pa * 4352 + m * 272 + k * 64 + g * 16), ldf(sm + A_V + pb * 4352 + m * 272 + k * 64 + g * 16), c);
    u32x4 u = ((__builtin_bit_cast(u32x4, c) + 0x26u) & 0xffffff00u) | 0x5au;
    if (valid) __builtin_amdgcn_raw_buffer_store_b128(u, grs, slot + rb * GP, 0, 16);
  }
  FENCE();
  u32x4 r[16];
  u32x2 x3v[3];
  const int sf = __shfl(slot, __ffsll((unsigned long long)__ballot(valid)) - 1), sl = valid ? slot : sf;
  if (!LAST) {
    WAITV(0);
#pragma unroll
    for (int p = 0; p < 16; ++p) r[p] = __builtin_amdgcn_raw_buffer_load_b128(grs, sl, p * GP, 16);
  }
  if (LAST) {
    WAITV(1); BAR();
    f32x4 y[2][3] = {};
#pragma unroll
    for (int i = 0; i < 9; ++i)
#pragma unroll
      for (int t = 0; t < 3; ++t) {
        bf16x8 qf = ldf(sm + A_QT + (t * 18 + 2 * i + kh) * 1024 + l16);
        y[0][t] = mfma(qf, xf[0][i], y[0][t]); y[1][t] = mfma(qf, xf[1][i], y[1][t]);
      }
    SB();
#pragma unroll
    for (int p = 0; p < 16; ++p) r[p] = __builtin_amdgcn_raw_buffer_load_b128(grs, sl, p * GP, 0);
    SB();
    char* ex = sm + A_PK;
#pragma unroll
    for (int t = 0; t < 3; ++t) *(f32x4*)(ex + (w * 3 + t) * 1024 + l16) = y[1][t];
    BAR();
#pragma unroll
    for (int t = 0; t < 3; ++t) {
      x3v[t] = pk4(y[0][t] + *(const f32x4*)(ex + ((w ^ 4) * 3 + t) * 1024 + l16));
      PIN(x3v[t]);
    }
  }
  FENCE();
  {
    for (int spin = 0; spin < 1000000; ++spin) {
      bool bad = false;
#pragma unroll
      for (int p = 0; p < 16; ++p)
        if (!__all(((r[p][0] & r[p][1]) & (r[p][2] & r[p][3]) & 0xffu) == 0x5au)) {
          FENCE();
          r[p] = __builtin_amdgcn_raw_buffer_load_b128(grs, sl, p * GP, 16);
          bad = true;
        }
      if (!bad) break;
    }
    if (valid) {
      f32x4 sum = Z4;
#pragma unroll
      for (int p = 0; p < 16; ++p) sum += __builtin_bit_cast(f32x4, r[p]);
      u32x2 u = pk4(sum);
      if (isz ? dq <= 4 : !eh) *(u32x2*)(sm + A_KV + (isz ? 18 : ec) * 64 + 16 + 8 * dq) = (u32x2){u[0], dq == 4 ? 0u : u[1]};
      if (isz ? dq >= 4 : eh) *(u32x2*)(sm + A_KV + (isz ? 50 : ec + 14) * 64 - 16 + 8 * dq) = (u32x2){dq == 4 ? 0u : u[0], u[1]};
    }
  }
  if (LAST) {
#pragma unroll
    for (int t = 0; t < 3; ++t)
      if (t < 2 || g == 0) { const int n = 36 * hp + 16 * t + 4 * g; St2{ws + O_X3 + ((n >> 3) * S + s0 + 16 * kh + m) * 16 + (n & 4) * 2} = x3v[t]; }
  }
  BAR(); STAMP(KID, 7);
  {
    unsigned tail[2];
    char* ob = ws + O_OB + (s0 + 16 * kh + m) * 16;
#pragma unroll
    for (int h = 0; h < 2; ++h) {
      bf16x8 k0 = ldf(sm + A_KV + (32 * h + m) * 64 + g * 16), k1 = ldf(sm + A_KV + (32 * h + 16 + m) * 64 + g * 16);
      bf16x8 pf = trf(sm + A_Q + w * 1536 + (16 * h + 8 * g + (m >> 2)) * 32 + (m & 3) * 8, 128);
      f32x4 n0 = mfma(k0, pf, Z4), n1 = mfma(k1, pf, Z4);
      float inv = __builtin_amdgcn_rcpf(__shfl(n1[2], m) + 1e-6f);
      int P = 9 * hp + 4 * h + g;
      St2{ob + (P >> 1) * (S * 16) + (P & 1) * 8} = pk4(n0 * inv);
      tail[h] = pk2(n1[0] * inv, n1[1] * inv);
    }
    int P = 9 * hp + 8;
    if (g == 0) St2{ob + (P >> 1) * (S * 16) + (P & 1) * 8} = (u32x2){tail[0], tail[1]};
  }
  FENCE(); STAMP(KID, 8);
}

template <int MODE>
DI void out_body(char* ws, char* outp) {
  extern __shared__ __attribute__((aligned(16))) char sm[];
  const int tid = threadIdx.x, w = __builtin_amdgcn_readfirstlane(tid >> 6), l = tid & 63, g = l >> 4, m = l & 15;
  const int b = blockIdx.x, rb = 8 * ((b & 7) >> 1) + ((b >> 3) & 7), ng = 3 * (b & 1) + (b >> 6);
  const int wr = w & 1, ch = (w >> 1) & 1, kh = w >> 2, s = 64 * rb + 32 * wr + 16 * kh + m;
  constexpr int KID = MODE == 2 ? 3 : 6;
  STAMP(KID, 0);
  const rsrc_t wrs = mkrs(ws + (MODE == 2 ? O_WOQ : O_WOF) + ng * 110592, 110592);
  const int l16 = l * 16, xo0 = (g * S + s) * 16, xo1 = xo0 ^ 256;
  bf16x8 xf[2][9];
  auto issue1 = [&](int st, int p) {
    if (p < 3) {
      if (st == 4 && p == 2) return;
      int q = w + 8 * p; if (st == 4) q = q < 12 ? q : q - 8;
      int pc = st < 4 ? (q >> 2) * 18 + 4 * st + (q & 3) : (q >> 1) * 18 + 16 + (q & 1);
      bglds(wrs, sm + pc * 1024, l16, pc * 1024);
    } else {
      const int i = 2 * st + ((p - 3) >> 1);
      if (i > 8) return;
      const char* xb = ws + O_OB + (2 * i + kh) * (4 * S * 16);
      if (p & 1) ALD16(xf[0][i], xo0, xb, 0); else ALD16(xf[1][i], xo1, xb, 0);
    }
    FENCE(); SB();
  };
  auto issue = [&](int st) {
#pragma unroll
    for (int p = 0; p < 7; ++p) issue1(st, p);
  };
  issue(0); issue(1);
  u32x2 rv[3];
  int ro[3];
#pragma unroll
  for (int t = 0; t < 3; ++t) {
    int n = 16 * (6 * ng + 3 * ch + t) + 4 * g;
    ro[t] = ((n >> 3) * S + s) * 16 + (n & 4) * 2;
  }
  STAMP(KID, 1);
  f32x4 acc[6] = {};
  bf16x8 wf[2][6];
  const char* wa = sm + (54 * ch + kh) * 1024 + l16;
  WAITV(7); BAR(); STAMP(KID, 2);
#pragma unroll
  for (int t = 0; t < 6; ++t) wf[0][t] = ldf(wa + ((t % 3) * 18 + 2 * (t / 3)) * 1024);
  issue(2);
#pragma unroll
  for (int st = 0; st < 5; ++st) {
    if (st < 4) {
      if (st < 3) WAITV(7); else WAITV(3);
      BAR();
    }
#pragma unroll
    for (int k = 0; k < (st < 4 ? 2 : 1); ++k) {
      PIN(xf[0][2 * st + k]); PIN(xf[1][2 * st + k]);
#pragma unroll
      for (int t = 0; t < 3; ++t) {
        acc[t] = mfma(wf[st & 1][3 * k + t], xf[0][2 * st + k], acc[t]); acc[3 + t] = mfma(wf[st & 1][3 * k + t], xf[1][2 * st + k], acc[3 + t]);
        SB();
        if (st < 3 || (st == 3 && k == 0)) wf[(st + 1) & 1][3 * k + t] = ldf(wa + (t * 18 + 4 * st + 4 + 2 * k) * 1024);
        SB();
        if (st < 2) { issue1(st + 3, 3 * k + t); if (k == 1 && t == 2) issue1(st + 3, 6); }
        if (st == 1 && k == 1 && t == 2) {
          const char* rp = ws + (MODE == 2 ? O_XH1 : O_X3);
          ALD8(rv[0], ro[0], rp); ALD8(rv[1], ro[1], rp); ALD8(rv[2], ro[2], rp);
        }
      }
    }
  }
  BAR(); STAMP(KID, 4);
#pragma unroll
  for (int t = 0; t < 3; ++t) *(f32x4*)(sm + (w * 3 + t) * 1024 + l16) = acc[3 + t];
  WAITV(0); BAR();
  char* op = MODE == 2 ? ws + O_XH2 : outp;
  float sq = 0.f;
#pragma unroll
  for (int t = 0; t < 3; ++t) {
    PIN(rv[t]);
    f32x4 v = acc[t] + *(const f32x4*)(sm + ((w ^ 4) * 3 + t) * 1024 + l16);
    v[0] += lo16(rv[t][0]); v[1] += hi16(rv[t][0]); v[2] += lo16(rv[t][1]); v[3] += hi16(rv[t][1]);
    St2{op + (MODE == 2 ? ro[t] : s * 1152 + (16 * (6 * ng + 3 * ch + t) + 4 * g) * 2)} = pk4(v);
    if (MODE == 2) sq += v[0] * v[0] + v[1] * v[1] + v[2] * v[2] + ((t == 2 && ng == 5 && ch && g == 3) ? 0.f : v[3] * v[3]);
  }
  if (MODE == 2) {
    sq += __shfl_xor(sq, 16); sq += __shfl_xor(sq, 32);
    float* red = (float*)(sm + 32768);
    if (ch && g == 0) red[(2 * wr + kh) * 16 + m] = sq;
    BAR();
    if (!ch && g == 0) ((float*)(ws + O_SS2))[ng * S + s] = sq + red[(2 * wr + kh) * 16 + m];
  }
  FENCE(); STAMP(KID, 5);
}

__global__ void __launch_bounds__(512, 2) k_attn0(char* ws) { attn_body<0>(ws + O_XH1, ws); }
__global__ void __launch_bounds__(512, 2) k_out2(char* ws) { out_body<2>(ws, nullptr); }
__global__ void __launch_bounds__(512, 2) k_attn1(char* ws) { attn_body<1>(ws + O_XH3, ws); }
__global__ void __launch_bounds__(512, 2) k_out3(char* ws, char* outp) { out_body<3>(ws, outp); }

__global__ void __launch_bounds__(768, 6) k_pre(const char* x0, const char* wo, const char* wr, const char* wi, const char* wqkv, char* ws) {
  extern __shared__ __attribute__((aligned(16))) char sm[];
  const int b = blockIdx.x, tid = threadIdx.x;
  if (b < 192) {
    int x = b & 7, i = b >> 3;
    gemm_q<0>(sm, 4 * x + (i & 3), i >> 2, x0, ws + O_XH0, (float*)(ws + O_SS0));
  } else if (b < 246) {
    int u = b - 192;
    gemm_q<1>(sm, u % 9, u / 9, wo, ws + O_WOQ, nullptr);
  } else {
    STAMP(0, 0);
    int u = b - 246, tg = u >> 3, kb = 8 * (u & 7);
    u32x4 v[4], code;
    u32x2 wpf;
    {
      const char* pc; int len;
      asm volatile("s_getpc_b64 %0\n.Lpre_pc:\n\ts_mov_b32 %1, .Lpre_pc-k_fft1" : "=s"(pc), "=s"(len));
      int off = tg * 12288 + tid * 16;
      off = off + 16 <= len ? off : 0;
      const char* base = len > 0 ? pc - len : pc;
      ALD16(code, off, base, 0);
      const int wq = (u * 243 + (tid < 243 ? tid : 0)) * 128;
      ALD8(wpf, wq, wqkv);
    }
#pragma unroll
    for (int i = 0; i < 4; ++i) {
      int e = i * 768 + tid; e = e < 2312 ? e : 2311;
      int run = e / 289, o = e - 289 * run;
      v[i] = *(const u32x4*)((run < 4 ? wr : wi) + (kb + 64 * (4 * tg + (run & 3))) * 578 + o * 16);
    }
    {
      const int id = u * 768 + tid;
      St4{ws + O_G + id * 16} = (u32x4){0u, 0u, 0u, 0u};
      if (id < 4608) St4{ws + O_WF + ((7 * (id / 288) + 6) * 18 + (id % 288 >> 4)) * 1024 + (12 + (id & 3) + 16 * ((id >> 2) & 3)) * 16} = (u32x4){0u, 0u, 0u, 0u};
    }
#pragma unroll
    for (int i = 0; i < 4; ++i) { int e = i * 768 + tid; e = e < 2312 ? e : 2311; *(u32x4*)(sm + e * 16) = v[i]; }
    __syncthreads();
    STAMP(0, 2);
#pragma unroll
    for (int i = 0; i < 3; ++i) {
      int e = i * 768 + tid, mm = e & 7, c = e >> 3;
      const char* p = sm + mm * 578 + 2 * (c + 1);
      u32x4 o;
#pragma unroll
      for (int r = 0; r < 4; ++r) o[r] = (unsigned)*(const u16*)(p + r * 4624) | ((unsigned)*(const u16*)(p + (4 + r) * 4624) << 16);
      St4{ws + O_WT + ((c * 4 + (kb >> 4)) * 2 + (tg >> 2)) * 1024 + (16 * (tg & 3) + (kb & 8) + mm) * 16} = o;
    }
    WAITV(0); PIN(code); PIN(wpf);
    FENCE(); STAMP(0, 5);
  }
}

extern "C" void kernel_launch(void* const* d_in, const int* in_sizes, int n_in, void* d_out, int out_size, void* d_ws,
                              size_t ws_size, hipStream_t stream) {
  const char *x0 = (const char*)d_in[0], *wr = (const char*)d_in[1], *wi = (const char*)d_in[2];
  const char *wqkv = (const char*)d_in[4], *wo = (const char*)d_in[5];
  char* ws = (char*)d_ws;
  (void)hipFuncSetAttribute((const void*)k_pre, hipFuncAttributeMaxDynamicSharedMemorySize, 73728);
  (void)hipFuncSetAttribute((const void*)k_fft1, hipFuncAttributeMaxDynamicSharedMemorySize, 73728);
  (void)hipFuncSetAttribute((const void*)k_attn0, hipFuncAttributeMaxDynamicSharedMemorySize, 163328);
  (void)hipFuncSetAttribute((const void*)k_attn1, hipFuncAttributeMaxDynamicSharedMemorySize, 163328);
  (void)hipFuncSetAttribute((const void*)k_out2, hipFuncAttributeMaxDynamicSharedMemorySize, 110592);
  (void)hipFuncSetAttribute((const void*)k_out3, hipFuncAttributeMaxDynamicSharedMemorySize, 110592);
  k_pre<<<310, 768, 73728, stream>>>(x0, wo, wr, wi, wqkv, ws);
  k_fft1<<<252, 512, 73728, stream>>>(wqkv, ws);
  k_attn0<<<256, 512, 163328, stream>>>(ws);
  k_out2<<<192, 512, 110592, stream>>>(ws);
  k_fft2<<<252, 512, 40960, stream>>>(wo, ws);
  k_attn1<<<256, 512, 163328, stream>>>(ws);
  k_out3<<<192, 512, 110592, stream>>>(ws, (char*)d_out);
}
```

```cpp
#include <hip/hip_runtime.h>
#include <hip/hip_bf16.h>

typedef __bf16 bf16x8 __attribute__((ext_vector_type(8)));
typedef __bf16 bf16x2 __attribute__((ext_vector_type(2)));
typedef short s16x4 __attribute__((ext_vector_type(4)));
typedef float f32x4 __attribute__((ext_vector_type(4)));
typedef float f32x2 __attribute__((ext_vector_type(2)));
typedef unsigned u32x2 __attribute__((ext_vector_type(2)));
typedef unsigned u32x4 __attribute__((ext_vector_type(4)));
typedef unsigned short u16;
#define LDSP __attribute__((address_space(3)))
#define GLBP __attribute__((address_space(1)))
#define DI __device__ __forceinline__
#define FENCE() asm volatile("" ::: "memory")
#define WAITV(n) asm volatile("s_waitcnt vmcnt(" #n ")" ::: "memory")
#define SB() __builtin_amdgcn_sched_barrier(0)
#define BAR() do { SB(); asm volatile("s_waitcnt lgkmcnt(0)" ::: "memory"); __builtin_amdgcn_s_barrier(); FENCE(); SB(); } while (0)

#define STAMP(k, i) do {} while (0)
constexpr int S = 2048, D = 576;
constexpr bool ALIAS = false;
constexpr size_t XB = (size_t)S * D * 2;
constexpr size_t O_XH0 = 0, O_XH1 = XB, O_XH2 = ALIAS ? 0 : 2 * XB, O_XH3 = ALIAS ? XB : 3 * XB, O_OB = (ALIAS ? 2 : 4) * XB;
constexpr size_t O_WT = (ALIAS ? 3 : 6) * XB, O_X3 = ALIAS ? O_WT : 5 * XB;
constexpr size_t O_WOQ = ALIAS ? 4 * XB : 7 * XB + 663552, O_WOF = O_WOQ + 663552, O_WF = O_WOF + 663552;
constexpr size_t O_QTF = O_WF + 2064384, O_SS0 = O_QTF + 884736, O_SS2 = O_SS0 + 8192, O_G = O_SS2 + 6 * 8192;
constexpr int GP = 3072;
constexpr size_t GL = (size_t)256 * GP;
constexpr size_t O_OB2 = O_G + 2 * GL;

DI f32x4 mfma(bf16x8 a, bf16x8 b, f32x4 c) { return __builtin_amdgcn_mfma_f32_16x16x32_bf16(a, b, c, 0, 0, 0); }
DI unsigned pk2(float a, float b) { f32x2 f = {a, b}; return __builtin_bit_cast(unsigned, __builtin_convertvector(f, bf16x2)); }
DI bf16x8 pk8(f32x4 a, f32x4 b) { u32x4 u = {pk2(a[0], a[1]), pk2(a[2], a[3]), pk2(b[0], b[1]), pk2(b[2], b[3])}; return __builtin_bit_cast(bf16x8, u); }
DI float lo16(unsigned u) { return __builtin_bit_cast(float, u << 16); }
DI float hi16(unsigned u) { return __builtin_bit_cast(float, u & 0xffff0000u); }
DI float cosr(float rev) { return __builtin_amdgcn_cosf(__builtin_amdgcn_fractf(rev)); }
DI float sinr(float rev) { return __builtin_amdgcn_sinf(__builtin_amdgcn_fractf(rev)); }
DI void glds(const char* g, char* l) { __builtin_amdgcn_global_load_lds((const GLBP void*)g, (LDSP void*)l, 16, 0, 0); }
DI bf16x8 ldf(const char* p) { return *(const bf16x8*)p; }
typedef __amdgpu_buffer_rsrc_t rsrc_t;
DI rsrc_t mkrs(const void* p, unsigned bytes) { return __builtin_amdgcn_make_buffer_rsrc((void*)p, 0, bytes, 0x00020000); }
DI void bglds(rsrc_t r, char* l, int voff, int soff) { __builtin_amdgcn_raw_ptr_buffer_load_lds(r, (LDSP void*)l, 16, voff, soff, 0, 0); }
#define ALD16(dst, voff, sbase, imm) asm volatile("global_load_dwordx4 %0, %1, %2 offset:" #imm : "=v"(dst) : "v"(voff), "s"(sbase))
#define ALD8(dst, voff, sbase) asm volatile("global_load_dwordx2 %0, %1, %2" : "=v"(dst) : "v"(voff), "s"(sbase))
#define PIN(x) asm volatile("" : "+v"(x))
DI bf16x8 trf(const char* p, int d) {
  s16x4 lo = __builtin_amdgcn_ds_read_tr16_b64_v4i16((LDSP s16x4*)p), hi = __builtin_amdgcn_ds_read_tr16_b64_v4i16((LDSP s16x4*)(p + d));
  return __builtin_bit_cast(bf16x8, __builtin_shufflevector(lo, hi, 0, 1, 2, 3, 4, 5, 6, 7));
}
struct St4 { char* p; DI void operator=(u32x4 v) const { asm volatile("global_store_dwordx4 %0, %1, off sc1\n\ts_nop 1" :: "v"(p), "v"(v) : "memory"); } };
struct St2 { char* p; DI void operator=(u32x2 v) const { *(u32x2*)p = v; } };
DI u32x2 pk4(f32x4 v) { return (u32x2){pk2(v[0], v[1]), pk2(v[2], v[3])}; }
constexpr u32x4 ONES = {0x3f803f80u, 0x3f803f80u, 0x3f803f80u, 0x3f803f80u};
constexpr f32x4 Z4 = {0.f, 0.f, 0.f, 0.f};
DI float qent(int d, int j) {
  int f = (j >> 1) + 1; float ph = (j & 1) ? 0.25f : 0.f, sc = 0.05892556509887896f;
  if (j >= 574) { sc = 0.041666666666666664f; ph = 0.f; f = (j == 575) ? 0 : 288; }
  return sc * cosr((float)(f * d) * (1.f / 576.f) + ph);
}
DI int operm(int k) {
  int hp = k / 36, c = k - 36 * hp, p = c >> 2, i = c & 3;
  return p < 8 ? 18 * (2 * hp + (p >> 2)) + 4 * (p & 3) + i : 18 * (2 * hp + (i >> 1)) + 16 + (i & 1);
}

DI void qgen(bf16x8* qf, int j, int d0, int dd) {
  int f = (j >> 1) + 1; float ph = (j & 1) ? 0.25f : 0.f, sc = 0.05892556509887896f;
  if (j >= 574) { sc = 0.041666666666666664f; ph = 0.f; f = (j == 575) ? 0 : 288; }
  const float st = (float)f * (1.f / 576.f), c2 = 2.f * __builtin_amdgcn_cosf(st), p0 = st * (float)d0 + ph;
#pragma unroll
  for (int i = 0; i < 9; ++i) {
    float p = __builtin_amdgcn_fractf(st * (float)(dd * i) + p0), v[8];
    v[0] = sc * __builtin_amdgcn_cosf(p); v[1] = sc * __builtin_amdgcn_cosf(p + st);
#pragma unroll
    for (int e = 2; e < 8; ++e) v[e] = c2 * v[e - 1] - v[e - 2];
    qf[i] = __builtin_bit_cast(bf16x8, (u32x4){pk2(v[0], v[1]), pk2(v[2], v[3]), pk2(v[4], v[5]), pk2(v[6], v[7])});
    PIN(qf[i]);
  }
}
template <int MODE>
DI void gemm_q(char* sm, int rb, int ng, const char* xs, char* out, float* ssq) {
  const int tid = threadIdx.x, w = __builtin_amdgcn_readfirstlane(tid >> 6), l = tid & 63, g = l >> 4, m = l & 15;
  const int kh = w >= 6, nt = w - 6 * kh, jt = 6 * ng + nt;
  STAMP(0, 0);
  {
    const rsrc_t xrs = mkrs(xs, MODE == 0 ? (unsigned)XB : 663552u);
    int voff[2];
#pragma unroll
    for (int h = 0; h < 2; ++h) {
      int r = 64 * rb + 8 * ((w + 4 * h) & 7) + (l >> 3);
      voff[h] = (MODE == 1 ? operm(r) : r) * 1152 + (((l & 7) ^ (l >> 3)) << 4);
    }
#pragma unroll
    for (int i = 0; i < 6; ++i) { int q = w + 12 * i; bglds(xrs, sm + q * 1024, voff[i & 1], (q >> 3) * 128); }
  }
  SB(); STAMP(0, 1);
  bf16x8 qf[9];
  qgen(qf, 16 * jt + m, 8 * g + 32 * kh, 64);
  WAITV(0); BAR(); STAMP(0, 2);
  f32x4 acc[4] = {}, gr = Z4, su = Z4;
  const bool stats = MODE == 0 && ng == 0 && nt < 4;
  const int x2 = 2 * kh;
  const char* xb = sm + (m >> 3) * 1024 + (m & 7) * 128 + (((4 * kh + g) ^ (m & 7)) << 4);
#pragma unroll
  for (int i = 0; i < 9; ++i) {
#pragma unroll
    for (int a = 0; a < 4; ++a) {
      bf16x8 xf = ldf(xb + i * 8192 + (a ^ x2) * 2048);
      acc[a] = MODE == 1 ? mfma(xf, qf[i], acc[a]) : mfma(qf[i], xf, acc[a]);
    }
    if (stats) { bf16x8 xf = ldf(xb + i * 8192 + nt * 2048); gr = mfma(xf, xf, gr); su = mfma(__builtin_bit_cast(bf16x8, ONES), xf, su); }
  }
  BAR(); STAMP(0, 4);
  *(f32x4*)(sm + w * 4096 + l * 16) = acc[2]; *(f32x4*)(sm + w * 4096 + 1024 + l * 16) = acc[3];
  if (stats && kh) { *(f32x4*)(sm + w * 4096 + 2048 + l * 16) = gr; *(f32x4*)(sm + w * 4096 + 3072 + l * 16) = su; }
  BAR();
  const char* pp = sm + (kh ? w - 6 : w + 6) * 4096 + l * 16;
#pragma unroll
  for (int a = 0; a < 2; ++a) {
    f32x4 v = acc[a] + *(const f32x4*)(pp + a * 1024);
    const int rt = 4 * rb + x2 + a, n = 16 * jt + 4 * g;
    if (MODE == 1) St2{out + (jt * 18 + (rt >> 1)) * 1024 + (m + 16 * (2 * (rt & 1) + (g >> 1))) * 16 + (g & 1) * 8} = pk4(v);
    else St2{out + ((n >> 3) * S + 16 * rt + m) * 16 + (n & 4) * 2} = pk4(v);
  }
  if (stats && !kh) {
    gr += *(const f32x4*)(pp + 2048); su += *(const f32x4*)(pp + 3072);
    float dsel = (m & 2) ? ((m & 1) ? gr[3] : gr[2]) : ((m & 1) ? gr[1] : gr[0]);
    if ((m >> 2) == g) ssq[64 * rb + 16 * nt + m] = fmaxf(dsel - su[0] * su[0] * (1.f / 576.f), 0.f);
  }
  FENCE(); STAMP(0, 5);
}

DI void fft_cols(char* sm, int b, const int second, const char* xin, char* xout, const float* ssq, const char* wt) {
  const int tid = threadIdx.x, w8 = __builtin_amdgcn_readfirstlane(tid >> 6), l = tid & 63, g = l >> 4, m = l & 15;
  const int w = w8 & 3, hi = w8 >> 2;
  const int kb = (b & 7) + 8 * (b >> 4), half = (b >> 3) & 1;
  const bool last = kb == 71 && half == 1;
  const int KID = second ? 4 : 1; (void)KID;
  STAMP(KID, 0);
  f32x4 ps[6];
#pragma unroll
  for (int p = 0; p < 6; ++p) ps[p] = *(const f32x4*)(ssq + p * (second ? S : 0) + tid * 4);
  u32x2 xv[4];
  const int xo = (kb * S + 32 * (16 * w + 4 * g) + 16 * hi + m) * 16 + 8 * half;
#pragma unroll
  for (int r = 0; r < 4; ++r) xv[r] = *(const u32x2*)(xin + xo + r * 512);
  u32x4 wv[2];
#pragma unroll
  for (int t = 0; t < 2; ++t) wv[t] = *(const u32x4*)(wt + (((4 * kb + 2 * half + hi) * 4 + w) * 2 + t) * 1024 + l * 16);
  FENCE(); STAMP(KID, 1);
  SB();
  bf16x8 c64[2], s64[2], n64[2], c32[2], s32[2], n32[2];
  f32x4 t1c[2], t1s[2], t2c[2], t2s[2];
  {
    const float k1 = (float)(16 * w + m), g4 = (float)(4 * g);
    const unsigned sg = (m & 1) ? 0x80008000u : 0u;
    float c[8], sn[8];
    const float a = k1 * (1.f / 64.f), a2 = 2.f * __builtin_amdgcn_cosf(a), p = a * (g4 + g4);
    c[0] = __builtin_amdgcn_cosf(p); c[1] = __builtin_amdgcn_cosf(p + a); sn[0] = __builtin_amdgcn_sinf(p); sn[1] = __builtin_amdgcn_sinf(p + a);
#pragma unroll
    for (int j = 2; j < 8; ++j) { c[j] = a2 * c[j - 1] - c[j - 2]; sn[j] = a2 * sn[j - 1] - sn[j - 2]; }
    u32x4 uc = {pk2(c[0], c[1]), pk2(c[2], c[3]), pk2(c[4], c[5]), pk2(c[6], c[7])}, us = {pk2(sn[0], sn[1]), pk2(sn[2], sn[3]), pk2(sn[4], sn[5]), pk2(sn[6], sn[7])};
    c64[0] = __builtin_bit_cast(bf16x8, uc); c64[1] = __builtin_bit_cast(bf16x8, uc ^ sg);
    s64[0] = __builtin_bit_cast(bf16x8, us); s64[1] = __builtin_bit_cast(bf16x8, us ^ sg);
    n64[0] = __builtin_bit_cast(bf16x8, us ^ 0x80008000u); n64[1] = __builtin_bit_cast(bf16x8, us ^ (sg ^ 0x80008000u));
    const float e = (float)m * (1.f / 32.f);
#pragma unroll
    for (int j = 0; j < 4; ++j) { float q = e * (g4 + (float)j); c[j] = __builtin_amdgcn_cosf(q); sn[j] = __builtin_amdgcn_sinf(q); }
    unsigned c0 = pk2(c[0], c[1]), c1 = pk2(c[2], c[3]), s0 = pk2(sn[0], sn[1]), s1 = pk2(sn[2], sn[3]);
    uc = (u32x4){c0, c1, c0 ^ sg, c1 ^ sg}; us = (u32x4){s0, s1, s0 ^ sg, s1 ^ sg};
    c32[0] = __builtin_bit_cast(bf16x8, uc); c32[1] = __builtin_bit_cast(bf16x8, uc ^ 0x80000000u);
    s32[0] = __builtin_bit_cast(bf16x8, us); s32[1] = __builtin_bit_cast(bf16x8, us ^ 0x80000000u);
    n32[0] = __builtin_bit_cast(bf16x8, us ^ 0x80008000u); n32[1] = __builtin_bit_cast(bf16x8, us ^ 0x00008000u);
    const float b1 = k1 * (1.f / 2048.f), b12 = 2.f * __builtin_amdgcn_cosf(b1);
#pragma unroll
    for (int h = 0; h < 2; ++h) {
      const float p1 = b1 * (g4 + (float)(16 * h)), b2 = (float)(16 * h + m) * (1.f / 2048.f), b22 = 2.f * __builtin_amdgcn_cosf(b2), p2 = b2 * (g4 + (float)(16 * w));
      t1c[h][0] = __builtin_amdgcn_cosf(p1); t1c[h][1] = __builtin_amdgcn_cosf(p1 + b1); t1s[h][0] = __builtin_amdgcn_sinf(p1); t1s[h][1] = __builtin_amdgcn_sinf(p1 + b1);
      t2c[h][0] = __builtin_amdgcn_cosf(p2); t2c[h][1] = __builtin_amdgcn_cosf(p2 + b2); t2s[h][0] = __builtin_amdgcn_sinf(p2); t2s[h][1] = __builtin_amdgcn_sinf(p2 + b2);
#pragma unroll
      for (int r = 2; r < 4; ++r) {
        t1c[h][r] = b12 * t1c[h][r - 1] - t1c[h][r - 2]; t1s[h][r] = b12 * t1s[h][r - 1] - t1s[h][r - 2];
        t2c[h][r] = b22 * t2c[h][r - 1] - t2c[h][r - 2]; t2s[h][r] = b22 * t2s[h][r - 1] - t2s[h][r - 2];
      }
      PIN(c64[h]); PIN(s64[h]); PIN(n64[h]); PIN(c32[h]); PIN(s32[h]); PIN(n32[h]); PIN(t1c[h]); PIN(t1s[h]); PIN(t2c[h]); PIN(t2s[h]);
    }
  }
  SB();
  FENCE(); STAMP(KID, 2);
  float* rsl = (float*)(sm + 32768);
  {
    f32x4 a = ps[0];
#pragma unroll
    for (int p = 1; p < 6; ++p) a += ps[p] * (second ? 1.f : 0.f);
#pragma unroll
    for (int i = 0; i < 4; ++i) a[i] = __builtin_amdgcn_rsqf(a[i] * (1.f / 576.f) + 1e-5f) * (1.f / 2048.f);
    *(f32x4*)(rsl + tid * 4) = a;
  }
  BAR();
  const int wo = m * 128 + (((2 * w + (g >> 1)) ^ (m & 7)) << 4) + (g & 1) * 8;
  {
    f32x4 u[4];
#pragma unroll
    for (int r = 0; r < 4; ++r) {
      float rs = rsl[32 * (16 * w + 4 * g + r) + 16 * hi + m];
      u[0][r] = lo16(xv[r][0]) * rs; u[1][r] = hi16(xv[r][0]) * rs; u[2][r] = lo16(xv[r][1]) * rs;
      u[3][r] = last ? 0.f : hi16(xv[r][1]) * rs;
    }
#pragma unroll
    for (int p = 0; p < 4; ++p) *(u32x2*)(sm + p * 4096 + hi * 2048 + wo) = pk4(u[p]);
  }
  BAR(); STAMP(KID, 3);
  const int ro0 = m * 128 + ((g ^ (m & 7)) << 4), ro1 = ro0 ^ 64;
  {
    f32x4 re[2], im[2];
#pragma unroll
    for (int t = 0; t < 2; ++t) {
      const char* p = sm + hi * 8192 + t * 2048;
      bf16x8 a0 = ldf(p + ro0), a1 = ldf(p + ro1), b0 = ldf(p + 4096 + ro0), b1 = ldf(p + 4096 + ro1);
      f32x4 x = mfma(a0, c64[0], Z4), y = mfma(b0, c64[0], Z4);
      x = mfma(a1, c64[1], x); y = mfma(b1, c64[1], y);
      x = mfma(b0, s64[0], x); y = mfma(a0, n64[0], y);
      x = mfma(b1, s64[1], x); y = mfma(a1, n64[1], y);
#pragma unroll
      for (int r = 0; r < 4; ++r) { re[t][r] = x[r] * t1c[t][r] + y[r] * t1s[t][r]; im[t][r] = y[r] * t1c[t][r] - x[r] * t1s[t][r]; }
    }
    bf16x8 fr = pk8(re[0], re[1]), fi = pk8(im[0], im[1]);
#pragma unroll
    for (int t = 0; t < 2; ++t) {
      f32x4 x = mfma(c32[t], fr, Z4), y = mfma(c32[t], fi, Z4);
      x = mfma(s32[t], fi, x); y = mfma(n32[t], fr, y);
#pragma unroll
      for (int r = 0; r < 4; ++r) {
        float a = lo16(wv[t][r]), bb = hi16(wv[t][r]);
        re[t][r] = x[r] * a - y[r] * bb; im[t][r] = x[r] * bb + y[r] * a;
      }
    }
    fr = pk8(re[0], re[1]); fi = pk8(im[0], im[1]);
#pragma unroll
    for (int t = 0; t < 2; ++t) {
      f32x4 x = mfma(fr, c32[t], Z4), y = mfma(fi, c32[t], Z4);
      x = mfma(fi, n32[t], x); y = mfma(fr, s32[t], y);
      f32x4 vr, vi;
#pragma unroll
      for (int r = 0; r < 4; ++r) { vr[r] = x[r] * t2c[t][r] - y[r] * t2s[t][r]; vi[r] = y[r] * t2c[t][r] + x[r] * t2s[t][r]; }
      char* p = sm + 16384 + hi * 8192 + t * 2048 + wo;
      *(u32x2*)p = pk4(vr); *(u32x2*)(p + 4096) = pk4(vi);
    }
  }
  BAR(); STAMP(KID, 4);
  {
    f32x4 yr[2], yi[2];
#pragma unroll
    for (int c = 0; c < 2; ++c) {
      const char* p = sm + 16384 + c * 8192 + hi * 2048;
      bf16x8 a0 = ldf(p + ro0), a1 = ldf(p + ro1), b0 = ldf(p + 4096 + ro0), b1 = ldf(p + 4096 + ro1);
      f32x4 x = mfma(c64[0], a0, Z4), y = mfma(c64[0], b0, Z4);
      x = mfma(c64[1], a1, x); y = mfma(c64[1], b1, y);
      x = mfma(n64[0], b0, x); y = mfma(s64[0], a0, y);
      x = mfma(n64[1], b1, x); y = mfma(s64[1], a1, y);
      yr[c] = x; yi[c] = y;
    }
#pragma unroll
    for (int r = 0; r < 4; ++r) {
      u32x2 o = {pk2(lo16(xv[r][0]) + yr[0][r], hi16(xv[r][0]) + yi[0][r]),
                 pk2(lo16(xv[r][1]) + yr[1][r], hi16(xv[r][1]) + (last ? 0.f : yi[1][r]))};
      St2{xout + xo + r * 512} = o;
    }
  }
  FENCE(); STAMP(KID, 5);
}

DI void wqkv_rot(char* sm, int u, const char* wqkv, char* wf) {
  const int tid = threadIdx.x, w = __builtin_amdgcn_readfirstlane(tid >> 6), l = tid & 63, g = l >> 4, m = l & 15;
  const int jg = u & 3, ng = u >> 2, kh = w >> 2, wc = w & 3, jt = 9 * jg + 2 * wc + kh, j8 = 9 * jg + 8;
  STAMP(1, 0);
  const rsrc_t rs = mkrs(wqkv, 1990656u);
  const int voff = (l >> 3) * 3456 + (((((l >> 1) & 3) ^ (((l >> 4) & 1) | ((w & 1) << 1))) << 5) | ((l & 1) << 4));
  const int l16 = l * 16;
#pragma unroll
  for (int i = 0; i < 9; ++i) bglds(rs, sm + (w + 8 * i) * 1024, voff, (w + 8 * i) * 27648 + ng * 128);
  SB();
  bf16x8 af[3][9];
  qgen(af[0], 16 * jt + m, 288 * kh + 8 * g, 32);
  qgen(af[1], 16 * (jt + 1 - 2 * kh) + m, 288 * kh + 8 * g, 32);
  qgen(af[2], 16 * j8 + m, 288 * kh + 8 * g, 32);
  int ob[4];
#pragma unroll
  for (int t = 0; t < 4; ++t) {
    int n = 64 * ng + 16 * (t ^ wc) + m, seg = (n >= 576) + (n >= 1152), c = n - 576 * seg, hp = c / 36;
    int cc = c - 36 * hp + (seg == 0 ? 72 : seg == 1 ? 0 : 36);
    ob[t] = (7 * hp + (cc >> 4)) * 18432 + (cc & 15) * 16;
  }
  STAMP(1, 2);
  WAITV(0); BAR(); STAMP(1, 3);
  f32x4 acc[2][4] = {}, a8 = Z4;
  const int rbase = 9 * kh * 4096 + (8 * g + (m >> 2)) * 128 + 8 * (m & 3), fx = (((m >> 3) & 1) | ((g & 1) << 1)) ^ wc;
#pragma unroll
  for (int i = 0; i < 9; ++i) {
#pragma unroll
    for (int t = 0; t < 4; ++t) {
      bf16x8 bfr = trf(sm + i * 4096 + rbase + ((t ^ fx) << 5), 512);
      acc[0][t] = mfma(af[0][i], bfr, acc[0][t]); acc[1][t] = mfma(af[1][i], bfr, acc[1][t]);
      if (t == 0) a8 = mfma(af[2][i], bfr, a8);
    }
  }
  BAR(); STAMP(1, 4);
#pragma unroll
  for (int t = 0; t < 4; ++t) *(f32x4*)(sm + (w * 5 + t) * 1024 + l16) = acc[1][t];
  *(f32x4*)(sm + (w * 5 + 4) * 1024 + l16) = a8;
  BAR();
  const int jo = (jt >> 1) * 1024 + (2 * (jt & 1) + (g >> 1)) * 256 + (g & 1) * 8;
#pragma unroll
  for (int t = 0; t < 4; ++t) St2{wf + ob[t] + jo} = pk4(acc[0][t] + *(const f32x4*)(sm + ((w ^ 4) * 5 + t) * 1024 + l16));
  if (!kh) {
    f32x4 o = a8 + *(const f32x4*)(sm + ((w ^ 4) * 5 + 4) * 1024 + l16);
    if (j8 == 35 && g == 3) o[3] = 0.f;
    St2{wf + ob[0] + (j8 >> 1) * 1024 + (2 * (j8 & 1) + (g >> 1)) * 256 + (g & 1) * 8} = pk4(o);
  }
  FENCE(); STAMP(1, 5);
}

extern "C" __global__ void __launch_bounds__(512, 2) k_fft1(const char* wsrc, char* ws, long layer) {
  extern __shared__ __attribute__((aligned(16))) char sm[];
  const int b = blockIdx.x, second = (int)layer;
  if (b < 144) fft_cols(sm, b, second, ws + (second ? O_XH2 : O_XH0), ws + (second ? O_XH3 : O_XH1), (const float*)(ws + (second ? O_SS2 : O_SS0)), ws + O_WT);
  else if (!second) wqkv_rot(sm, b - 144, wsrc, ws + O_WF);
  else {
    const char* wo = wsrc;
    const int e = (b - 144) * 512 + threadIdx.x, l = e & 63, pc = e >> 6, nt = pc / 18, ks = pc - 18 * nt;
    unsigned h[8];
    if (e < 41472) {
      const int r0 = operm(32 * ks + 8 * (l >> 4)), r1 = operm(32 * ks + 8 * (l >> 4) + 4), c0 = (32 * ks + 8 * (l >> 4)) % 36;
#pragma unroll
      for (int j = 0; j < 8; ++j) {
        int r = (j < 4 ? r0 : r1) + (c0 == (j < 4 ? 32 : 28) ? 18 * ((j >> 1) & 1) + (j & 1) : (j & 3));
        h[j] = *(const u16*)(wo + r * 1152 + (16 * nt + (l & 15)) * 2);
      }
    }
    if (e < (int)(GL / 16)) St4{ws + O_G + GL + e * 16} = (u32x4){0u, 0u, 0u, 0u};
    const int hp = nt / 3, c = 16 * (nt - 3 * hp) + (l & 15), d = 36 * hp + (c < 36 ? c : 0), j0 = 32 * ks + 8 * (l >> 4);
    St4{ws + O_QTF + e * 16} = (u32x4){pk2(qent(d, j0), qent(d, j0 + 1)), pk2(qent(d, j0 + 2), qent(d, j0 + 3)),
                                             pk2(qent(d, j0 + 4), qent(d, j0 + 5)), pk2(qent(d, j0 + 6), qent(d, j0 + 7))};
    if (e < 41472) St4{ws + O_WOF + e * 16} = (u32x4){h[0] | (h[1] << 16), h[2] | (h[3] << 16), h[4] | (h[5] << 16), h[6] | (h[7] << 16)};
  }
}

constexpr int A_QT = 103936, A_KV = A_QT + 55296;
constexpr int A_PK = 65536, A_V = A_PK + 13056, A_Q = A_V + 13056;
__global__ void __launch_bounds__(512, 2) k_attn(char* ws, long layer) {
  const int LAST = (int)layer;
  const char* xin = ws + (LAST ? O_XH3 : O_XH1);
  extern __shared__ __attribute__((aligned(16))) char sm[];
  const int tid = threadIdx.x, w = __builtin_amdgcn_readfirstlane(tid >> 6), l = tid & 63, g = l >> 4, m = l & 15;
  const int b = blockIdx.x, rb = 4 * ((b & 7) >> 1) + ((b >> 3) & 3), hp = 8 * (b & 1) + (b >> 5);
  const int wr = w & 3, kh = w >> 2, s0 = 128 * rb + 32 * wr;
  const int KID = LAST ? 5 : 2; (void)KID;
  STAMP(KID, 0);
  const rsrc_t wrs = mkrs(ws + O_WF + hp * 129024, 129024);
  const int l16 = l * 16, xo0 = (g * S + s0 + 16 * kh + m) * 16, xo1 = xo0 ^ 256;
  bf16x8 xf[2][9];
  auto issue1 = [&](int j, int p) {
    if (p < 2) {
      int q = w + 8 * p; q = q < 14 ? q : q - 8;
      int pc = (q >> 1) * 18 + 2 * j + (q & 1);
      bglds(wrs, sm + pc * 1024, l16, pc * 1024);
    } else {
      const char* xb = xin + (2 * j + kh) * (4 * S * 16);
      if (p == 2) ALD16(xf[0][j], xo0, xb, 0); else ALD16(xf[1][j], xo1, xb, 0);
    }
    FENCE(); SB();
  };
  auto issue = [&](int j) { issue1(j, 0); issue1(j, 1); issue1(j, 2); issue1(j, 3); };
#pragma unroll
  for (int j = 0; j < 3; ++j) issue(j);
  if (tid < 256) *(u32x4*)(sm + A_KV + tid * 16) = (u32x4){0u, 0u, 0u, 0u};
  const int pa = w < 6 ? w / 3 : 2, pb = w < 6 ? w - 3 * pa : w - 5;
  const int dq = 4 * pa + g, ec = 16 * pb + m - 4, eh = ec >= 18;
  const bool isz = ec == 36, valid = isz ? dq < 9 : (ec >= 0 && ec < 36 && dq >= 4 * eh && dq <= 4 * eh + 4);
  const int slot = (isz ? 180 + dq : 36 * (dq - 4 * eh) + ec) * 16;
  STAMP(KID, 1);
  f32x4 acc[2][7] = {};
  float sq[2] = {0.f, 0.f};
  bf16x8 wf[2][7];
  WAITV(8); BAR(); STAMP(KID, 2);
#pragma unroll
  for (int t = 0; t < 7; ++t) wf[0][t] = ldf(sm + (t * 18 + kh) * 1024 + l16);
  issue(3);
#pragma unroll
  for (int i = 0; i < 9; ++i) {
    if (i < 8) {
      if (i <= 5) WAITV(8); else if (i == 6) WAITV(4); else WAITV(0);
      BAR();
    }
    PIN(xf[0][i]); PIN(xf[1][i]);
#pragma unroll
    for (int t = 0; t < 7; ++t) {
      acc[0][t] = mfma(xf[0][i], wf[i & 1][t], acc[0][t]); acc[1][t] = mfma(xf[1][i], wf[i & 1][t], acc[1][t]);
      if (t < 4) {
#pragma unroll
        for (int a = 0; a < 2; ++a) {
          unsigned u = __builtin_bit_cast(u32x4, xf[a][i])[t];
          if (i == 8 && t == 3) u = (g == 3 && kh) ? (u & 0xffffu) : u;
          bf16x2 h = __builtin_bit_cast(bf16x2, u);
          sq[a] = __builtin_amdgcn_fdot2_f32_bf16(h, h, sq[a], false);
        }
      }
      SB();
      if (i < 8) wf[(i + 1) & 1][t] = ldf(sm + (t * 18 + 2 * i + 2 + kh) * 1024 + l16);
      SB();
      if (!(t & 1) && i + 4 < 9) issue1(i + 4, t >> 1);
    }
  }
  BAR(); STAMP(KID, 3);
  if (LAST) {
    const rsrc_t qrs = mkrs(ws + O_QTF + hp * 55296, 55296);
#pragma unroll
    for (int i = 0; i < 7; ++i) { int q = w + 8 * i; q = q < 54 ? q : q - 8; bglds(qrs, sm + A_QT + q * 1024, l16, q * 1024); }
  }
  SB();
#pragma unroll
  for (int t = 0; t < 7; ++t) *(f32x4*)(sm + (w * 8 + t) * 1024 + l16) = acc[1][t];
  *(float*)(sm + (w * 8 + 7) * 1024 + l * 4) = sq[1];
  BAR();
  float rq = sq[0] + *(const float*)(sm + ((w ^ 4) * 8 + 7) * 1024 + l * 4);
  f32x4 v[7];
#pragma unroll
  for (int t = 0; t < 7; ++t) v[t] = acc[0][t] + *(const f32x4*)(sm + ((w ^ 4) * 8 + t) * 1024 + l16);
  rq += __shfl_xor(rq, 16); rq += __shfl_xor(rq, 32);
  rq = __builtin_amdgcn_rsqf(rq * (1.f / 576.f) + 1e-5f);
  {
    f32x4 rs, rl;
#pragma unroll
    for (int r = 0; r < 4; ++r) { rs[r] = __shfl(rq, 4 * g + r); rl[r] = rs[r] * 1.4426950408889634f; }
    const int so = (32 * wr + 16 * kh + 4 * g) * 2 + m * 272;
#pragma unroll
    for (int t = 0; t < 7; ++t) {
      if (t != 3) {
        f32x4 p;
        const f32x4 z = v[t] * rl, y = __builtin_elementwise_fma(v[t], rs, (f32x4){1e-6f, 1e-6f, 1e-6f, 1e-6f});
        f32x4 e;
#pragma unroll
        for (int r = 0; r < 4; ++r) { e[r] = __builtin_amdgcn_fmed3f(__builtin_amdgcn_exp2f(z[r]), 0.f, 1.f); p[r] = fmaxf(y[r], 1e-6f); }
        p += e;
        if (t < 3) *(u32x2*)(sm + A_PK + t * 4352 + so) = pk4(p);
        else *(u32x2*)(sm + A_Q + w * 1536 + (16 * (t - 4) + m) * 32 + g * 8) = pk4(p);
      }
      if (t >= 2 && t <= 4) {
        u32x2 u = pk4(v[t] * rs);
        if (t == 4 && m == 8) u = (u32x2){0x3f803f80u, 0x3f803f80u};
        *(u32x2*)(sm + A_V + (t - 2) * 4352 + so) = u;
      }
    }
  }
  BAR(); STAMP(KID, 4);
  const rsrc_t grs = mkrs(ws + O_G + (LAST ? GL : 0) + hp * 16 * GP, 16 * GP);
  {
    f32x4 c = Z4;
#pragma unroll
    for (int k = 0; k < 4; ++k)
      c = mfma(ldf(sm + A_PK + pa * 4352 + m * 272 + k * 64 + g * 16), ldf(sm + A_V + pb * 4352 + m * 272 + k * 64 + g * 16), c);
    u32x4 u = ((__builtin_bit_cast(u32x4, c) + 0x26u) & 0xffffff00u) | 0x5au;
    if (valid) __builtin_amdgcn_raw_buffer_store_b128(u, grs, slot + rb * GP, 0, 16);
  }
  FENCE();
  u32x4 r[16];
  u32x2 x3v[3];
  const int sf = __shfl(slot, __ffsll((unsigned long long)__ballot(valid)) - 1), sl = valid ? slot : sf;
  if (!LAST) {
    WAITV(0);
#pragma unroll
    for (int p = 0; p < 16; ++p) r[p] = __builtin_amdgcn_raw_buffer_load_b128(grs, sl, p * GP, 16);
  }
  if (LAST) {
    WAITV(1); BAR();
    f32x4 y[2][3] = {};
#pragma unroll
    for (int i = 0; i < 9; ++i)
#pragma unroll
      for (int t = 0; t < 3; ++t) {
        bf16x8 qf = ldf(sm + A_QT + (t * 18 + 2 * i + kh) * 1024 + l16);
        y[0][t] = mfma(qf, xf[0][i], y[0][t]); y[1][t] = mfma(qf, xf[1][i], y[1][t]);
      }
    SB();
#pragma unroll
    for (int p = 0; p < 16; ++p) r[p] = __builtin_amdgcn_raw_buffer_load_b128(grs, sl, p * GP, 0);
    SB();
    char* ex = sm + A_PK;
#pragma unroll
    for (int t = 0; t < 3; ++t) *(f32x4*)(ex + (w * 3 + t) * 1024 + l16) = y[1][t];
    BAR();
#pragma unroll
    for (int t = 0; t < 3; ++t) {
      x3v[t] = pk4(y[0][t] + *(const f32x4*)(ex + ((w ^ 4) * 3 + t) * 1024 + l16));
      PIN(x3v[t]);
    }
  }
  FENCE();
  {
    for (int spin = 0; spin < 1000000; ++spin) {
      bool bad = false;
#pragma unroll
      for (int p = 0; p < 16; ++p)
        if (!__all(((r[p][0] & r[p][1]) & (r[p][2] & r[p][3]) & 0xffu) == 0x5au)) {
          FENCE();
          r[p] = __builtin_amdgcn_raw_buffer_load_b128(grs, sl, p * GP, 16);
          bad = true;
        }
      if (!bad) break;
    }
    if (valid) {
      f32x4 sum = Z4;
#pragma unroll
      for (int p = 0; p < 16; ++p) sum += __builtin_bit_cast(f32x4, r[p]);
      u32x2 u = pk4(sum);
      if (isz ? dq <= 4 : !eh) *(u32x2*)(sm + A_KV + (isz ? 18 : ec) * 64 + 16 + 8 * dq) = (u32x2){u[0], dq == 4 ? 0u : u[1]};
      if (isz ? dq >= 4 : eh) *(u32x2*)(sm + A_KV + (isz ? 50 : ec + 14) * 64 - 16 + 8 * dq) = (u32x2){dq == 4 ? 0u : u[0], u[1]};
    }
  }
  if (LAST) {
#pragma unroll
    for (int t = 0; t < 3; ++t)
      if (t < 2 || g == 0) { const int n = 36 * hp + 16 * t + 4 * g; St2{ws + O_X3 + ((n >> 3) * S + s0 + 16 * kh + m) * 16 + (n & 4) * 2} = x3v[t]; }
  }
  BAR(); STAMP(KID, 7);
  {
    unsigned tail[2];
    char* ob = ws + (LAST ? O_OB2 : O_OB) + (s0 + 16 * kh + m) * 16;
#pragma unroll
    for (int h = 0; h < 2; ++h) {
      bf16x8 k0 = ldf(sm + A_KV + (32 * h + m) * 64 + g * 16), k1 = ldf(sm + A_KV + (32 * h + 16 + m) * 64 + g * 16);
      bf16x8 pf = trf(sm + A_Q + w * 1536 + (16 * h + 8 * g + (m >> 2)) * 32 + (m & 3) * 8, 128);
      f32x4 n0 = mfma(k0, pf, Z4), n1 = mfma(k1, pf, Z4);
      float inv = __builtin_amdgcn_rcpf(__shfl(n1[2], m) + 1e-6f);
      int P = 9 * hp + 4 * h + g;
      St2{ob + (P >> 1) * (S * 16) + (P & 1) * 8} = pk4(n0 * inv);
      tail[h] = pk2(n1[0] * inv, n1[1] * inv);
    }
    int P = 9 * hp + 8;
    if (g == 0) St2{ob + (P >> 1) * (S * 16) + (P & 1) * 8} = (u32x2){tail[0], tail[1]};
  }
  FENCE(); STAMP(KID, 8);
}

__global__ void __launch_bounds__(512, 2) k_out(char* ws, char* outp, long layer) {
  const int MODE = 2 + (int)layer;
  const char* obp = ws + (MODE == 2 ? O_OB : O_OB2);
  extern __shared__ __attribute__((aligned(16))) char sm[];
  const int tid = threadIdx.x, w = __builtin_amdgcn_readfirstlane(tid >> 6), l = tid & 63, g = l >> 4, m = l & 15;
  const int b = blockIdx.x, rb = 8 * ((b & 7) >> 1) + ((b >> 3) & 7), ng = 3 * (b & 1) + (b >> 6);
  const int wr = w & 1, ch = (w >> 1) & 1, kh = w >> 2, s = 64 * rb + 32 * wr + 16 * kh + m;
  const int KID = MODE == 2 ? 3 : 6; (void)KID;
  STAMP(KID, 0);
  const rsrc_t wrs = mkrs(ws + (MODE == 2 ? O_WOQ : O_WOF) + ng * 110592, 110592);
  const int l16 = l * 16, xo0 = (g * S + s) * 16, xo1 = xo0 ^ 256;
  bf16x8 xf[2][9];
  auto issue1 = [&](int st, int p) {
    if (p < 3) {
      if (st == 4 && p == 2) return;
      int q = w + 8 * p; if (st == 4) q = q < 12 ? q : q - 8;
      int pc = st < 4 ? (q >> 2) * 18 + 4 * st + (q & 3) : (q >> 1) * 18 + 16 + (q & 1);
      bglds(wrs, sm + pc * 1024, l16, pc * 1024);
    } else {
      const int i = 2 * st + ((p - 3) >> 1);
      if (i > 8) return;
      const char* xb = obp + (2 * i + kh) * (4 * S * 16);
      if (p & 1) ALD16(xf[0][i], xo0, xb, 0); else ALD16(xf[1][i], xo1, xb, 0);
    }
    FENCE(); SB();
  };
  auto issue = [&](int st) {
#pragma unroll
    for (int p = 0; p < 7; ++p) issue1(st, p);
  };
  issue(0); issue(1);
  u32x2 rv[3];
  int ro[3];
#pragma unroll
  for (int t = 0; t < 3; ++t) {
    int n = 16 * (6 * ng + 3 * ch + t) + 4 * g;
    ro[t] = ((n >> 3) * S + s) * 16 + (n & 4) * 2;
  }
  STAMP(KID, 1);
  f32x4 acc[6] = {};
  bf16x8 wf[2][6];
  const char* wa = sm + (54 * ch + kh) * 1024 + l16;
  WAITV(7); BAR(); STAMP(KID, 2);
#pragma unroll
  for (int t = 0; t < 6; ++t) wf[0][t] = ldf(wa + ((t % 3) * 18 + 2 * (t / 3)) * 1024);
  issue(2);
#pragma unroll
  for (int st = 0; st < 5; ++st) {
    if (st < 4) {
      if (st < 3) WAITV(7); else WAITV(3);
      BAR();
    }
#pragma unroll
    for (int k = 0; k < (st < 4 ? 2 : 1); ++k) {
      PIN(xf[0][2 * st + k]); PIN(xf[1][2 * st + k]);
#pragma unroll
      for (int t = 0; t < 3; ++t) {
        acc[t] = mfma(wf[st & 1][3 * k + t], xf[0][2 * st + k], acc[t]); acc[3 + t] = mfma(wf[st & 1][3 * k + t], xf[1][2 * st + k], acc[3 + t]);
        SB();
        if (st < 3 || (st == 3 && k == 0)) wf[(st + 1) & 1][3 * k + t] = ldf(wa + (t * 18 + 4 * st + 4 + 2 * k) * 1024);
        SB();
        if (st < 2) { issue1(st + 3, 3 * k + t); if (k == 1 && t == 2) issue1(st + 3, 6); }
        if (st == 1 && k == 1 && t == 2) {
          const char* rp = ws + (MODE == 2 ? O_XH1 : O_X3);
          ALD8(rv[0], ro[0], rp); ALD8(rv[1], ro[1], rp); ALD8(rv[2], ro[2], rp);
        }
      }
    }
  }
  BAR(); STAMP(KID, 4);
#pragma unroll
  for (int t = 0; t < 3; ++t) *(f32x4*)(sm + (w * 3 + t) * 1024 + l16) = acc[3 + t];
  WAITV(0); BAR();
  char* op = MODE == 2 ? ws + O_XH2 : outp;
  float sq = 0.f;
#pragma unroll
  for (int t = 0; t < 3; ++t) {
    PIN(rv[t]);
    f32x4 v = acc[t] + *(const f32x4*)(sm + ((w ^ 4) * 3 + t) * 1024 + l16);
    v[0] += lo16(rv[t][0]); v[1] += hi16(rv[t][0]); v[2] += lo16(rv[t][1]); v[3] += hi16(rv[t][1]);
    St2{op + (MODE == 2 ? ro[t] : s * 1152 + (16 * (6 * ng + 3 * ch + t) + 4 * g) * 2)} = pk4(v);
    if (MODE == 2) sq += v[0] * v[0] + v[1] * v[1] + v[2] * v[2] + ((t == 2 && ng == 5 && ch && g == 3) ? 0.f : v[3] * v[3]);
  }
  if (MODE == 2) {
    sq += __shfl_xor(sq, 16); sq += __shfl_xor(sq, 32);
    float* red = (float*)(sm + 32768);
    if (ch && g == 0) red[(2 * wr + kh) * 16 + m] = sq;
    BAR();
    if (!ch && g == 0) ((float*)(ws + O_SS2))[ng * S + s] = sq + red[(2 * wr + kh) * 16 + m];
  }
  FENCE(); STAMP(KID, 5);
}

__global__ void __launch_bounds__(768, 6) k_pre(const char* x0, const char* wo, const char* wr, const char* wi, const char* wqkv, char* ws) {
  extern __shared__ __attribute__((aligned(16))) char sm[];
  const int b = blockIdx.x, tid = threadIdx.x;
  if (b < 192) {
    int x = b & 7, i = b >> 3;
    gemm_q<0>(sm, 4 * x + (i & 3), i >> 2, x0, ws + O_XH0, (float*)(ws + O_SS0));
  } else if (b < 246) {
    int u = b - 192;
    gemm_q<1>(sm, u % 9, u / 9, wo, ws + O_WOQ, nullptr);
  } else {
    STAMP(0, 0);
    int u = b - 246, tg = u >> 3, kb = 8 * (u & 7);
    u32x4 v[4], code;
    u32x2 wpf;
    {
      const char* pc; int len;
      asm volatile("s_getpc_b64 %0\n.Lpre_pc:\n\ts_mov_b32 %1, .Lpre_pc-k_fft1" : "=s"(pc), "=s"(len));
      int off = tg * 12288 + tid * 16;
      off = off + 16 <= len ? off : 0;
      const char* base = len > 0 ? pc - len : pc;
      ALD16(code, off, base, 0);
      const int wq = (u * 243 + (tid < 243 ? tid : 0)) * 128;
      ALD8(wpf, wq, wqkv);
    }
#pragma unroll
    for (int i = 0; i < 4; ++i) {
      int e = i * 768 + tid; e = e < 2312 ? e : 2311;
      int run = e / 289, o = e - 289 * run;
      v[i] = *(const u32x4*)((run < 4 ? wr : wi) + (kb + 64 * (4 * tg + (run & 3))) * 578 + o * 16);
    }
    {
      const int id = u * 768 + tid;
      St4{ws + O_G + id * 16} = (u32x4){0u, 0u, 0u, 0u};
      if (id < 4608) St4{ws + O_WF + ((7 * (id / 288) + 6) * 18 + (id % 288 >> 4)) * 1024 + (12 + (id & 3) + 16 * ((id >> 2) & 3)) * 16} = (u32x4){0u, 0u, 0u, 0u};
    }
#pragma unroll
    for (int i = 0; i < 4; ++i) { int e = i * 768 + tid; e = e < 2312 ? e : 2311; *(u32x4*)(sm + e * 16) = v[i]; }
    __syncthreads();
    STAMP(0, 2);
#pragma unroll
    for (int i = 0; i < 3; ++i) {
      int e = i * 768 + tid, mm = e & 7, c = e >> 3;
      const char* p = sm + mm * 578 + 2 * (c + 1);
      u32x4 o;
#pragma unroll
      for (int r = 0; r < 4; ++r) o[r] = (unsigned)*(const u16*)(p + r * 4624) | ((unsigned)*(const u16*)(p + (4 + r) * 4624) << 16);
      St4{ws + O_WT + ((c * 4 + (kb >> 4)) * 2 + (tg >> 2)) * 1024 + (16 * (tg & 3) + (kb & 8) + mm) * 16} = o;
    }
    WAITV(0); PIN(code); PIN(wpf);
    FENCE(); STAMP(0, 5);
  }
}

extern "C" void kernel_launch(void* const* d_in, const int* in_sizes, int n_in, void* d_out, int out_size, void* d_ws,
                              size_t ws_size, hipStream_t stream) {
  const char *x0 = (const char*)d_in[0], *wr = (const char*)d_in[1], *wi = (const char*)d_in[2];
  const char *wqkv = (const char*)d_in[4], *wo = (const char*)d_in[5];
  char* ws = (char*)d_ws;
  (void)hipFuncSetAttribute((const void*)k_pre, hipFuncAttributeMaxDynamicSharedMemorySize, 73728);
  (void)hipFuncSetAttribute((const void*)k_fft1, hipFuncAttributeMaxDynamicSharedMemorySize, 73728);
  (void)hipFuncSetAttribute((const void*)k_attn, hipFuncAttributeMaxDynamicSharedMemorySize, 163328);
  (void)hipFuncSetAttribute((const void*)k_out, hipFuncAttributeMaxDynamicSharedMemorySize, 110592);
  k_pre<<<310, 768, 73728, stream>>>(x0, wo, wr, wi, wqkv, ws);
  k_fft1<<<252, 512, 73728, stream>>>(wqkv, ws, 0L);
  k_attn<<<256, 512, 163328, stream>>>(ws, 0L);
  k_out<<<192, 512, 110592, stream>>>(ws, (char*)d_out, 0L);
  k_fft1<<<252, 512, 40960, stream>>>(wo, ws, 1L);
  k_attn<<<256, 512, 163328, stream>>>(ws, 1L);
  k_out<<<192, 512, 110592, stream>>>(ws, (char*)d_out, 1L);
}
```

```cpp
#include <hip/hip_runtime.h>
#include <hip/hip_bf16.h>

typedef __bf16 bf16x8 __attribute__((ext_vector_type(8)));
typedef __bf16 bf16x2 __attribute__((ext_vector_type(2)));
typedef short s16x4 __attribute__((ext_vector_type(4)));
typedef float f32x4 __attribute__((ext_vector_type(4)));
typedef float f32x2 __attribute__((ext_vector_type(2)));
typedef unsigned u32x2 __attribute__((ext_vector_type(2)));
typedef unsigned u32x4 __attribute__((ext_vector_type(4)));
typedef unsigned short u16;
#define LDSP __attribute__((address_space(3)))
#define GLBP __attribute__((address_space(1)))
#define DI __device__ __forceinline__
#define FENCE() asm volatile("" ::: "memory")
#define WAITV(n) asm volatile("s_waitcnt vmcnt(" #n ")" ::: "memory")
#define SB() __builtin_amdgcn_sched_barrier(0)
#define BAR() do { SB(); asm volatile("s_waitcnt lgkmcnt(0)" ::: "memory"); __builtin_amdgcn_s_barrier(); FENCE(); SB(); } while (0)

#define STAMP(k, i) do {} while (0)
constexpr int S = 2048, D = 576;
constexpr bool ALIAS = false;
constexpr size_t XB = (size_t)S * D * 2;
constexpr size_t O_XH0 = 0, O_XH1 = XB, O_XH2 = ALIAS ? 0 : 2 * XB, O_XH3 = ALIAS ? XB : 3 * XB, O_OB = (ALIAS ? 2 : 4) * XB;
constexpr size_t O_WT = (ALIAS ? 3 : 6) * XB, O_X3 = ALIAS ? O_WT : 5 * XB;
constexpr size_t O_WOQ = ALIAS ? 4 * XB : 7 * XB + 663552, O_WOF = O_WOQ + 663552, O_WF = O_WOF + 663552;
constexpr size_t O_QTF = O_WF + 2064384, O_SS0 = O_QTF + 884736, O_SS2 = O_SS0 + 8192, O_G = O_SS2 + 6 * 8192;
constexpr int GP = 3072;
constexpr size_t GL = (size_t)256 * GP;
constexpr size_t O_OB2 = O_G + 2 * GL;

DI f32x4 mfma(bf16x8 a, bf16x8 b, f32x4 c) { return __builtin_amdgcn_mfma_f32_16x16x32_bf16(a, b, c, 0, 0, 0); }
DI unsigned pk2(float a, float b) { f32x2 f = {a, b}; return __builtin_bit_cast(unsigned, __builtin_convertvector(f, bf16x2)); }
DI bf16x8 pk8(f32x4 a, f32x4 b) { u32x4 u = {pk2(a[0], a[1]), pk2(a[2], a[3]), pk2(b[0], b[1]), pk2(b[2], b[3])}; return __builtin_bit_cast(bf16x8, u); }
DI float lo16(unsigned u) { return __builtin_bit_cast(float, u << 16); }
DI float hi16(unsigned u) { return __builtin_bit_cast(float, u & 0xffff0000u); }
DI float cosr(float rev) { return __builtin_amdgcn_cosf(__builtin_amdgcn_fractf(rev)); }
DI float sinr(float rev) { return __builtin_amdgcn_sinf(__builtin_amdgcn_fractf(rev)); }
DI void glds(const char* g, char* l) { __builtin_amdgcn_global_load_lds((const GLBP void*)g, (LDSP void*)l, 16, 0, 0); }
DI bf16x8 ldf(const char* p) { return *(const bf16x8*)p; }
typedef __amdgpu_buffer_rsrc_t rsrc_t;
DI rsrc_t mkrs(const void* p, unsigned bytes) { return __builtin_amdgcn_make_buffer_rsrc((void*)p, 0, bytes, 0x00020000); }
DI void bglds(rsrc_t r, char* l, int voff, int soff) { __builtin_amdgcn_raw_ptr_buffer_load_lds(r, (LDSP void*)l, 16, voff, soff, 0, 0); }
#define ALD16(dst, voff, sbase, imm) asm volatile("global_load_dwordx4 %0, %1, %2 offset:" #imm : "=v"(dst) : "v"(voff), "s"(sbase))
#define ALD8(dst, voff, sbase) asm volatile("global_load_dwordx2 %0, %1, %2" : "=v"(dst) : "v"(voff), "s"(sbase))
#define PIN(x) asm volatile("" : "+v"(x))
DI bf16x8 trf(const char* p, int d) {
  s16x4 lo = __builtin_amdgcn_ds_read_tr16_b64_v4i16((LDSP s16x4*)p), hi = __builtin_amdgcn_ds_read_tr16_b64_v4i16((LDSP s16x4*)(p + d));
  return __builtin_bit_cast(bf16x8, __builtin_shufflevector(lo, hi, 0, 1, 2, 3, 4, 5, 6, 7));
}
struct St4 { char* p; DI void operator=(u32x4 v) const { asm volatile("global_store_dwordx4 %0, %1, off sc1\n\ts_nop 1" :: "v"(p), "v"(v) : "memory"); } };
struct St2 { char* p; DI void operator=(u32x2 v) const { asm volatile("global_store_dwordx2 %0, %1, off sc1" :: "v"(p), "v"(v) : "memory"); } };
struct Sp2 { char* p; DI void operator=(u32x2 v) const { *(u32x2*)p = v; } };
DI u32x2 pk4(f32x4 v) { return (u32x2){pk2(v[0], v[1]), pk2(v[2], v[3])}; }
constexpr u32x4 ONES = {0x3f803f80u, 0x3f803f80u, 0x3f803f80u, 0x3f803f80u};
constexpr f32x4 Z4 = {0.f, 0.f, 0.f, 0.f};
DI float qent(int d, int j) {
  int f = (j >> 1) + 1; float ph = (j & 1) ? 0.25f : 0.f, sc = 0.05892556509887896f;
  if (j >= 574) { sc = 0.041666666666666664f; ph = 0.f; f = (j == 575) ? 0 : 288; }
  return sc * cosr((float)(f * d) * (1.f / 576.f) + ph);
}
DI int operm(int k) {
  int hp = k / 36, c = k - 36 * hp, p = c >> 2, i = c & 3;
  return p < 8 ? 18 * (2 * hp + (p >> 2)) + 4 * (p & 3) + i : 18 * (2 * hp + (i >> 1)) + 16 + (i & 1);
}

DI void qgen(bf16x8* qf, int j, int d0, int dd) {
  int f = (j >> 1) + 1; float ph = (j & 1) ? 0.25f : 0.f, sc = 0.05892556509887896f;
  if (j >= 574) { sc = 0.041666666666666664f; ph = 0.f; f = (j == 575) ? 0 : 288; }
  const float st = (float)f * (1.f / 576.f), c2 = 2.f * __builtin_amdgcn_cosf(st), p0 = st * (float)d0 + ph;
  const float cd = 2.f * cosr(st * (float)dd);
  f32x2 u[2][4];
#pragma unroll
  for (int i = 0; i < 9; ++i) {
    if (i < 2) {
      float p = __builtin_amdgcn_fractf(st * (float)(dd * i) + p0), v[8];
      v[0] = sc * __builtin_amdgcn_cosf(p); v[1] = sc * __builtin_amdgcn_cosf(p + st);
#pragma unroll
      for (int e = 2; e < 8; ++e) v[e] = c2 * v[e - 1] - v[e - 2];
#pragma unroll
      for (int e = 0; e < 4; ++e) u[i][e] = (f32x2){v[2 * e], v[2 * e + 1]};
    } else {
#pragma unroll
      for (int e = 0; e < 4; ++e) u[i & 1][e] = __builtin_elementwise_fma((f32x2){cd, cd}, u[(i & 1) ^ 1][e], -u[i & 1][e]);
    }
    const f32x2* x = u[i & 1];
    qf[i] = __builtin_bit_cast(bf16x8, (u32x4){pk2(x[0][0], x[0][1]), pk2(x[1][0], x[1][1]), pk2(x[2][0], x[2][1]), pk2(x[3][0], x[3][1])});
    PIN(qf[i]);
  }
}
template <int MODE>
DI void gemm_q(char* sm, int rb, int ng, const char* xs, char* out, float* ssq) {
  const int tid = threadIdx.x, w = __builtin_amdgcn_readfirstlane(tid >> 6), l = tid & 63, g = l >> 4, m = l & 15;
  const int kh = w >= 6, nt = w - 6 * kh, jt = 6 * ng + nt;
  STAMP(0, 0);
  {
    const rsrc_t xrs = mkrs(xs, MODE == 0 ? (unsigned)XB : 663552u);
    int voff[2];
#pragma unroll
    for (int h = 0; h < 2; ++h) {
      int r = 64 * rb + 8 * ((w + 4 * h) & 7) + (l >> 3);
      voff[h] = (MODE == 1 ? operm(r) : r) * 1152 + (((l & 7) ^ (l >> 3)) << 4);
    }
#pragma unroll
    for (int i = 0; i < 6; ++i) { int q = w + 12 * i; bglds(xrs, sm + q * 1024, voff[i & 1], (q >> 3) * 128); }
  }
  SB(); STAMP(0, 1);
  bf16x8 qf[9];
  qgen(qf, 16 * jt + m, 8 * g + 32 * kh, 64);
  WAITV(0); BAR(); STAMP(0, 2);
  f32x4 acc[4] = {}, gr = Z4, su = Z4;
  const bool stats = MODE == 0 && ng == 0 && nt < 4;
  const int x2 = 2 * kh;
  const char* xb = sm + (m >> 3) * 1024 + (m & 7) * 128 + (((4 * kh + g) ^ (m & 7)) << 4);
#pragma unroll
  for (int i = 0; i < 9; ++i) {
#pragma unroll
    for (int a = 0; a < 4; ++a) {
      bf16x8 xf = ldf(xb + i * 8192 + (a ^ x2) * 2048);
      acc[a] = MODE == 1 ? mfma(xf, qf[i], acc[a]) : mfma(qf[i], xf, acc[a]);
    }
    if (stats) { bf16x8 xf = ldf(xb + i * 8192 + nt * 2048); gr = mfma(xf, xf, gr); su = mfma(__builtin_bit_cast(bf16x8, ONES), xf, su); }
  }
  BAR(); STAMP(0, 4);
  *(f32x4*)(sm + w * 4096 + l * 16) = acc[2]; *(f32x4*)(sm + w * 4096 + 1024 + l * 16) = acc[3];
  if (stats && kh) { *(f32x4*)(sm + w * 4096 + 2048 + l * 16) = gr; *(f32x4*)(sm + w * 4096 + 3072 + l * 16) = su; }
  BAR();
  const char* pp = sm + (kh ? w - 6 : w + 6) * 4096 + l * 16;
#pragma unroll
  for (int a = 0; a < 2; ++a) {
    f32x4 v = acc[a] + *(const f32x4*)(pp + a * 1024);
    const int rt = 4 * rb + x2 + a, n = 16 * jt + 4 * g;
    if (MODE == 1) St2{out + (jt * 18 + (rt >> 1)) * 1024 + (m + 16 * (2 * (rt & 1) + (g >> 1))) * 16 + (g & 1) * 8} = pk4(v);
    else St2{out + ((n >> 3) * S + 16 * rt + m) * 16 + (n & 4) * 2} = pk4(v);
  }
  if (stats && !kh) {
    gr += *(const f32x4*)(pp + 2048); su += *(const f32x4*)(pp + 3072);
    float dsel = (m & 2) ? ((m & 1) ? gr[3] : gr[2]) : ((m & 1) ? gr[1] : gr[0]);
    if ((m >> 2) == g) ssq[64 * rb + 16 * nt + m] = fmaxf(dsel - su[0] * su[0] * (1.f / 576.f), 0.f);
  }
  FENCE(); STAMP(0, 5);
}

DI void fft_cols(char* sm, int b, const int second, const char* xin, char* xout, const float* ssq, const char* wt) {
  const int tid = threadIdx.x, w8 = __builtin_amdgcn_readfirstlane(tid >> 6), l = tid & 63, g = l >> 4, m = l & 15;
  const int w = w8 & 3, hi = w8 >> 2;
  const int kb = (b & 7) + 8 * (b >> 4), half = (b >> 3) & 1;
  const bool last = kb == 71 && half == 1;
  const int KID = second ? 4 : 1; (void)KID;
  STAMP(KID, 0);
  f32x4 ps[6];
#pragma unroll
  for (int p = 0; p < 6; ++p) ps[p] = *(const f32x4*)(ssq + p * (second ? S : 0) + tid * 4);
  u32x2 xv[4];
  const int xo = (kb * S + 32 * (16 * w + 4 * g) + 16 * hi + m) * 16 + 8 * half;
#pragma unroll
  for (int r = 0; r < 4; ++r) xv[r] = *(const u32x2*)(xin + xo + r * 512);
  u32x4 wv[2];
#pragma unroll
  for (int t = 0; t < 2; ++t) wv[t] = *(const u32x4*)(wt + (((4 * kb + 2 * half + hi) * 4 + w) * 2 + t) * 1024 + l * 16);
  FENCE(); STAMP(KID, 1);
  SB();
  bf16x8 c64[2], s64[2], n64[2], c32[2], s32[2], n32[2];
  f32x4 t1c[2], t1s[2], t2c[2], t2s[2];
  {
    const float k1 = (float)(16 * w + m), g4 = (float)(4 * g);
    const unsigned sg = (m & 1) ? 0x80008000u : 0u;
    float c[8], sn[8];
    const float a = k1 * (1.f / 64.f), a2 = 2.f * __builtin_amdgcn_cosf(a), p = a * (g4 + g4);
    c[0] = __builtin_amdgcn_cosf(p); c[1] = __builtin_amdgcn_cosf(p + a); sn[0] = __builtin_amdgcn_sinf(p); sn[1] = __builtin_amdgcn_sinf(p + a);
#pragma unroll
    for (int j = 2; j < 8; ++j) { c[j] = a2 * c[j - 1] - c[j - 2]; sn[j] = a2 * sn[j - 1] - sn[j - 2]; }
    u32x4 uc = {pk2(c[0], c[1]), pk2(c[2], c[3]), pk2(c[4], c[5]), pk2(c[6], c[7])}, us = {pk2(sn[0], sn[1]), pk2(sn[2], sn[3]), pk2(sn[4], sn[5]), pk2(sn[6], sn[7])};
    c64[0] = __builtin_bit_cast(bf16x8, uc); c64[1] = __builtin_bit_cast(bf16x8, uc ^ sg);
    s64[0] = __builtin_bit_cast(bf16x8, us); s64[1] = __builtin_bit_cast(bf16x8, us ^ sg);
    n64[0] = __builtin_bit_cast(bf16x8, us ^ 0x80008000u); n64[1] = __builtin_bit_cast(bf16x8, us ^ (sg ^ 0x80008000u));
    const float e = (float)m * (1.f / 32.f);
#pragma unroll
    for (int j = 0; j < 4; ++j) { float q = e * (g4 + (float)j); c[j] = __builtin_amdgcn_cosf(q); sn[j] = __builtin_amdgcn_sinf(q); }
    unsigned c0 = pk2(c[0], c[1]), c1 = pk2(c[2], c[3]), s0 = pk2(sn[0], sn[1]), s1 = pk2(sn[2], sn[3]);
    uc = (u32x4){c0, c1, c0 ^ sg, c1 ^ sg}; us = (u32x4){s0, s1, s0 ^ sg, s1 ^ sg};
    c32[0] = __builtin_bit_cast(bf16x8, uc); c32[1] = __builtin_bit_cast(bf16x8, uc ^ 0x80000000u);
    s32[0] = __builtin_bit_cast(bf16x8, us); s32[1] = __builtin_bit_cast(bf16x8, us ^ 0x80000000u);
    n32[0] = __builtin_bit_cast(bf16x8, us ^ 0x80008000u); n32[1] = __builtin_bit_cast(bf16x8, us ^ 0x00008000u);
    const float b1 = k1 * (1.f / 2048.f), b12 = 2.f * __builtin_amdgcn_cosf(b1);
#pragma unroll
    for (int h = 0; h < 2; ++h) {
      const float p1 = b1 * (g4 + (float)(16 * h)), b2 = (float)(16 * h + m) * (1.f / 2048.f), b22 = 2.f * __builtin_amdgcn_cosf(b2), p2 = b2 * (g4 + (float)(16 * w));
      t1c[h][0] = __builtin_amdgcn_cosf(p1); t1c[h][1] = __builtin_amdgcn_cosf(p1 + b1); t1s[h][0] = __builtin_amdgcn_sinf(p1); t1s[h][1] = __builtin_amdgcn_sinf(p1 + b1);
      t2c[h][0] = __builtin_amdgcn_cosf(p2); t2c[h][1] = __builtin_amdgcn_cosf(p2 + b2); t2s[h][0] = __builtin_amdgcn_sinf(p2); t2s[h][1] = __builtin_amdgcn_sinf(p2 + b2);
#pragma unroll
      for (int r = 2; r < 4; ++r) {
        t1c[h][r] = b12 * t1c[h][r - 1] - t1c[h][r - 2]; t1s[h][r] = b12 * t1s[h][r - 1] - t1s[h][r - 2];
        t2c[h][r] = b22 * t2c[h][r - 1] - t2c[h][r - 2]; t2s[h][r] = b22 * t2s[h][r - 1] - t2s[h][r - 2];
      }
      PIN(c64[h]); PIN(s64[h]); PIN(n64[h]); PIN(c32[h]); PIN(s32[h]); PIN(n32[h]); PIN(t1c[h]); PIN(t1s[h]); PIN(t2c[h]); PIN(t2s[h]);
    }
  }
  SB();
  FENCE(); STAMP(KID, 2);
  float* rsl = (float*)(sm + 32768);
  {
    f32x4 a = ps[0];
#pragma unroll
    for (int p = 1; p < 6; ++p) a += ps[p] * (second ? 1.f : 0.f);
#pragma unroll
    for (int i = 0; i < 4; ++i) a[i] = __builtin_amdgcn_rsqf(a[i] * (1.f / 576.f) + 1e-5f) * (1.f / 2048.f);
    *(f32x4*)(rsl + tid * 4) = a;
  }
  BAR();
  const int wo = m * 128 + (((2 * w + (g >> 1)) ^ (m & 7)) << 4) + (g & 1) * 8;
  {
    f32x4 u[4];
#pragma unroll
    for (int r = 0; r < 4; ++r) {
      float rs = rsl[32 * (16 * w + 4 * g + r) + 16 * hi + m];
      u[0][r] = lo16(xv[r][0]) * rs; u[1][r] = hi16(xv[r][0]) * rs; u[2][r] = lo16(xv[r][1]) * rs;
      u[3][r] = last ? 0.f : hi16(xv[r][1]) * rs;
    }
#pragma unroll
    for (int p = 0; p < 4; ++p) *(u32x2*)(sm + p * 4096 + hi * 2048 + wo) = pk4(u[p]);
  }
  BAR(); STAMP(KID, 3);
  const int ro0 = m * 128 + ((g ^ (m & 7)) << 4), ro1 = ro0 ^ 64;
  {
    f32x4 re[2], im[2];
#pragma unroll
    for (int t = 0; t < 2; ++t) {
      const char* p = sm + hi * 8192 + t * 2048;
      bf16x8 a0 = ldf(p + ro0), a1 = ldf(p + ro1), b0 = ldf(p + 4096 + ro0), b1 = ldf(p + 4096 + ro1);
      f32x4 x = mfma(a0, c64[0], Z4), y = mfma(b0, c64[0], Z4);
      x = mfma(a1, c64[1], x); y = mfma(b1, c64[1], y);
      x = mfma(b0, s64[0], x); y = mfma(a0, n64[0], y);
      x = mfma(b1, s64[1], x); y = mfma(a1, n64[1], y);
#pragma unroll
      for (int r = 0; r < 4; ++r) { re[t][r] = x[r] * t1c[t][r] + y[r] * t1s[t][r]; im[t][r] = y[r] * t1c[t][r] - x[r] * t1s[t][r]; }
    }
    bf16x8 fr = pk8(re[0], re[1]), fi = pk8(im[0], im[1]);
#pragma unroll
    for (int t = 0; t < 2; ++t) {
      f32x4 x = mfma(c32[t], fr, Z4), y = mfma(c32[t], fi, Z4);
      x = mfma(s32[t], fi, x); y = mfma(n32[t], fr, y);
#pragma unroll
      for (int r = 0; r < 4; ++r) {
        float a = lo16(wv[t][r]), bb = hi16(wv[t][r]);
        re[t][r] = x[r] * a - y[r] * bb; im[t][r] = x[r] * bb + y[r] * a;
      }
    }
    fr = pk8(re[0], re[1]); fi = pk8(im[0], im[1]);
#pragma unroll
    for (int t = 0; t < 2; ++t) {
      f32x4 x = mfma(fr, c32[t], Z4), y = mfma(fi, c32[t], Z4);
      x = mfma(fi, n32[t], x); y = mfma(fr, s32[t], y);
      f32x4 vr, vi;
#pragma unroll
      for (int r = 0; r < 4; ++r) { vr[r] = x[r] * t2c[t][r] - y[r] * t2s[t][r]; vi[r] = y[r] * t2c[t][r] + x[r] * t2s[t][r]; }
      char* p = sm + 16384 + hi * 8192 + t * 2048 + wo;
      *(u32x2*)p = pk4(vr); *(u32x2*)(p + 4096) = pk4(vi);
    }
  }
  BAR(); STAMP(KID, 4);
  {
    f32x4 yr[2], yi[2];
#pragma unroll
    for (int c = 0; c < 2; ++c) {
      const char* p = sm + 16384 + c * 8192 + hi * 2048;
      bf16x8 a0 = ldf(p + ro0), a1 = ldf(p + ro1), b0 = ldf(p + 4096 + ro0), b1 = ldf(p + 4096 + ro1);
      f32x4 x = mfma(c64[0], a0, Z4), y = mfma(c64[0], b0, Z4);
      x = mfma(c64[1], a1, x); y = mfma(c64[1], b1, y);
      x = mfma(n64[0], b0, x); y = mfma(s64[0], a0, y);
      x = mfma(n64[1], b1, x); y = mfma(s64[1], a1, y);
      yr[c] = x; yi[c] = y;
    }
#pragma unroll
    for (int r = 0; r < 4; ++r) {
      u32x2 o = {pk2(lo16(xv[r][0]) + yr[0][r], hi16(xv[r][0]) + yi[0][r]),
                 pk2(lo16(xv[r][1]) + yr[1][r], hi16(xv[r][1]) + (last ? 0.f : yi[1][r]))};
      Sp2{xout + xo + r * 512} = o;
    }
  }
  FENCE(); STAMP(KID, 5);
}

DI void wqkv_rot(char* sm, int u, const char* wqkv, char* wf) {
  const int tid = threadIdx.x, w = __builtin_amdgcn_readfirstlane(tid >> 6), l = tid & 63, g = l >> 4, m = l & 15;
  const int jg = u & 3, ng = u >> 2, kh = w >> 2, wc = w & 3, jt = 9 * jg + 2 * wc + kh, j8 = 9 * jg + 8;
  STAMP(1, 0);
  const rsrc_t rs = mkrs(wqkv, 1990656u);
  const int voff = (l >> 3) * 3456 + (((((l >> 1) & 3) ^ (((l >> 4) & 1) | ((w & 1) << 1))) << 5) | ((l & 1) << 4));
  const int l16 = l * 16;
#pragma unroll
  for (int i = 0; i < 9; ++i) bglds(rs, sm + (w + 8 * i) * 1024, voff, (w + 8 * i) * 27648 + ng * 128);
  SB();
  bf16x8 af[3][9];
  qgen(af[0], 16 * jt + m, 288 * kh + 8 * g, 32);
  qgen(af[1], 16 * (jt + 1 - 2 * kh) + m, 288 * kh + 8 * g, 32);
  qgen(af[2], 16 * j8 + m, 288 * kh + 8 * g, 32);
  int ob[4];
#pragma unroll
  for (int t = 0; t < 4; ++t) {
    int n = 64 * ng + 16 * (t ^ wc) + m, seg = (n >= 576) + (n >= 1152), c = n - 576 * seg, hp = c / 36;
    int cc = c - 36 * hp + (seg == 0 ? 72 : seg == 1 ? 0 : 36);
    ob[t] = (7 * hp + (cc >> 4)) * 18432 + (cc & 15) * 16;
  }
  STAMP(1, 2);
  WAITV(0); BAR(); STAMP(1, 3);
  f32x4 acc[2][4] = {}, a8 = Z4;
  const int rbase = 9 * kh * 4096 + (8 * g + (m >> 2)) * 128 + 8 * (m & 3), fx = (((m >> 3) & 1) | ((g & 1) << 1)) ^ wc;
#pragma unroll
  for (int i = 0; i < 9; ++i) {
#pragma unroll
    for (int t = 0; t < 4; ++t) {
      bf16x8 bfr = trf(sm + i * 4096 + rbase + ((t ^ fx) << 5), 512);
      acc[0][t] = mfma(af[0][i], bfr, acc[0][t]); acc[1][t] = mfma(af[1][i], bfr, acc[1][t]);
      if (t == 0) a8 = mfma(af[2][i], bfr, a8);
    }
  }
  BAR(); STAMP(1, 4);
#pragma unroll
  for (int t = 0; t < 4; ++t) *(f32x4*)(sm + (w * 5 + t) * 1024 + l16) = acc[1][t];
  *(f32x4*)(sm + (w * 5 + 4) * 1024 + l16) = a8;
  BAR();
  const int jo = (jt >> 1) * 1024 + (2 * (jt & 1) + (g >> 1)) * 256 + (g & 1) * 8;
#pragma unroll
  for (int t = 0; t < 4; ++t) St2{wf + ob[t] + jo} = pk4(acc[0][t] + *(const f32x4*)(sm + ((w ^ 4) * 5 + t) * 1024 + l16));
  if (!kh) {
    f32x4 o = a8 + *(const f32x4*)(sm + ((w ^ 4) * 5 + 4) * 1024 + l16);
    if (j8 == 35 && g == 3) o[3] = 0.f;
    St2{wf + ob[0] + (j8 >> 1) * 1024 + (2 * (j8 & 1) + (g >> 1)) * 256 + (g & 1) * 8} = pk4(o);
  }
  FENCE(); STAMP(1, 5);
}

extern "C" __global__ void __launch_bounds__(512, 2) k_fft1(const char* wsrc, char* ws, long layer) {
  extern __shared__ __attribute__((aligned(16))) char sm[];
  const int b = blockIdx.x, second = (int)layer;
  if (b < 144) fft_cols(sm, b, second, ws + (second ? O_XH2 : O_XH0), ws + (second ? O_XH3 : O_XH1), (const float*)(ws + (second ? O_SS2 : O_SS0)), ws + O_WT);
  else if (!second) wqkv_rot(sm, b - 144, wsrc, ws + O_WF);
  else {
    const char* wo = wsrc;
    const int e = (b - 144) * 512 + threadIdx.x, l = e & 63, pc = e >> 6, nt = pc / 18, ks = pc - 18 * nt;
    unsigned h[8];
    if (e < 41472) {
      const int r0 = operm(32 * ks + 8 * (l >> 4)), r1 = operm(32 * ks + 8 * (l >> 4) + 4), c0 = (32 * ks + 8 * (l >> 4)) % 36;
#pragma unroll
      for (int j = 0; j < 8; ++j) {
        int r = (j < 4 ? r0 : r1) + (c0 == (j < 4 ? 32 : 28) ? 18 * ((j >> 1) & 1) + (j & 1) : (j & 3));
        h[j] = *(const u16*)(wo + r * 1152 + (16 * nt + (l & 15)) * 2);
      }
    }
    if (e < (int)(GL / 16)) St4{ws + O_G + GL + e * 16} = (u32x4){0u, 0u, 0u, 0u};
    const int hp = nt / 3, c = 16 * (nt - 3 * hp) + (l & 15), d = 36 * hp + (c < 36 ? c : 0), j0 = 32 * ks + 8 * (l >> 4);
    St4{ws + O_QTF + e * 16} = (u32x4){pk2(qent(d, j0), qent(d, j0 + 1)), pk2(qent(d, j0 + 2), qent(d, j0 + 3)),
                                             pk2(qent(d, j0 + 4), qent(d, j0 + 5)), pk2(qent(d, j0 + 6), qent(d, j0 + 7))};
    if (e < 41472) St4{ws + O_WOF + e * 16} = (u32x4){h[0] | (h[1] << 16), h[2] | (h[3] << 16), h[4] | (h[5] << 16), h[6] | (h[7] << 16)};
  }
}

constexpr int A_QT = 103936, A_KV = A_QT + 55296;
constexpr int A_PK = 65536, A_V = A_PK + 13056, A_Q = A_V + 13056;
__global__ void __launch_bounds__(512, 2) k_attn(char* ws, long layer) {
  const int LAST = (int)layer;
  const char* xin = ws + (LAST ? O_XH3 : O_XH1);
  extern __shared__ __attribute__((aligned(16))) char sm[];
  const int tid = threadIdx.x, w = __builtin_amdgcn_readfirstlane(tid >> 6), l = tid & 63, g = l >> 4, m = l & 15;
  const int b = blockIdx.x, rb = 4 * ((b & 7) >> 1) + ((b >> 3) & 3), hp = 8 * (b & 1) + (b >> 5);
  const int wr = w & 3, kh = w >> 2, s0 = 128 * rb + 32 * wr;
  const int KID = LAST ? 5 : 2; (void)KID;
  STAMP(KID, 0);
  const rsrc_t wrs = mkrs(ws + O_WF + hp * 129024, 129024);
  const int l16 = l * 16, xo0 = (g * S + s0 + 16 * kh + m) * 16, xo1 = xo0 ^ 256;
  bf16x8 xf[2][9];
  auto issue1 = [&](int j, int p) {
    if (p < 2) {
      int q = w + 8 * p; q = q < 14 ? q : q - 8;
      int pc = (q >> 1) * 18 + 2 * j + (q & 1);
      bglds(wrs, sm + pc * 1024, l16, pc * 1024);
    } else {
      const char* xb = xin + (2 * j + kh) * (4 * S * 16);
      if (p == 2) ALD16(xf[0][j], xo0, xb, 0); else ALD16(xf[1][j], xo1, xb, 0);
    }
    FENCE(); SB();
  };
  auto issue = [&](int j) { issue1(j, 0); issue1(j, 1); issue1(j, 2); issue1(j, 3); };
#pragma unroll
  for (int j = 0; j < 3; ++j) issue(j);
  if (tid < 256) *(u32x4*)(sm + A_KV + tid * 16) = (u32x4){0u, 0u, 0u, 0u};
  const int pa = w < 6 ? w / 3 : 2, pb = w < 6 ? w - 3 * pa : w - 5;
  const int dq = 4 * pa + g, ec = 16 * pb + m - 4, eh = ec >= 18;
  const bool isz = ec == 36, valid = isz ? dq < 9 : (ec >= 0 && ec < 36 && dq >= 4 * eh && dq <= 4 * eh + 4);
  const int slot = (isz ? 180 + dq : 36 * (dq - 4 * eh) + ec) * 16;
  STAMP(KID, 1);
  f32x4 acc[2][7] = {};
  float sq[2] = {0.f, 0.f};
  bf16x8 wf[2][7];
  WAITV(8); BAR(); STAMP(KID, 2);
#pragma unroll
  for (int t = 0; t < 7; ++t) wf[0][t] = ldf(sm + (t * 18 + kh) * 1024 + l16);
  issue(3);
#pragma unroll
  for (int i = 0; i < 9; ++i) {
    if (i < 8) {
      if (i <= 5) WAITV(8); else if (i == 6) WAITV(4); else WAITV(0);
      BAR();
    }
    PIN(xf[0][i]); PIN(xf[1][i]);
#pragma unroll
    for (int t = 0; t < 7; ++t) {
      acc[0][t] = mfma(xf[0][i], wf[i & 1][t], acc[0][t]); acc[1][t] = mfma(xf[1][i], wf[i & 1][t], acc[1][t]);
      if (t < 4) {
#pragma unroll
        for (int a = 0; a < 2; ++a) {
          unsigned u = __builtin_bit_cast(u32x4, xf[a][i])[t];
          if (i == 8 && t == 3) u = (g == 3 && kh) ? (u & 0xffffu) : u;
          bf16x2 h = __builtin_bit_cast(bf16x2, u);
          sq[a] = __builtin_amdgcn_fdot2_f32_bf16(h, h, sq[a], false);
        }
      }
      SB();
      if (i < 8) wf[(i + 1) & 1][t] = ldf(sm + (t * 18 + 2 * i + 2 + kh) * 1024 + l16);
      SB();
      if (!(t & 1) && i + 4 < 9) issue1(i + 4, t >> 1);
    }
  }
  BAR(); STAMP(KID, 3);
  if (LAST) {
    const rsrc_t qrs = mkrs(ws + O_QTF + hp * 55296, 55296);
#pragma unroll
    for (int i = 0; i < 7; ++i) { int q = w + 8 * i; q = q < 54 ? q : q - 8; bglds(qrs, sm + A_QT + q * 1024, l16, q * 1024); }
  }
  SB();
#pragma unroll
  for (int t = 0; t < 7; ++t) *(f32x4*)(sm + (w * 8 + t) * 1024 + l16) = acc[1][t];
  *(float*)(sm + (w * 8 + 7) * 1024 + l * 4) = sq[1];
  BAR();
  float rq = sq[0] + *(const float*)(sm + ((w ^ 4) * 8 + 7) * 1024 + l * 4);
  f32x4 v[7];
#pragma unroll
  for (int t = 0; t < 7; ++t) v[t] = acc[0][t] + *(const f32x4*)(sm + ((w ^ 4) * 8 + t) * 1024 + l16);
  rq += __shfl_xor(rq, 16); rq += __shfl_xor(rq, 32);
  rq = __builtin_amdgcn_rsqf(rq * (1.f / 576.f) + 1e-5f);
  {
    f32x4 rs, rl;
#pragma unroll
    for (int r = 0; r < 4; ++r) { rs[r] = __shfl(rq, 4 * g + r); rl[r] = rs[r] * 1.4426950408889634f; }
    const int so = (32 * wr + 16 * kh + 4 * g) * 2 + m * 272;
#pragma unroll
    for (int t = 0; t < 7; ++t) {
      if (t != 3) {
        f32x4 p;
        const f32x4 z = v[t] * rl, y = __builtin_elementwise_fma(v[t], rs, (f32x4){1e-6f, 1e-6f, 1e-6f, 1e-6f});
        f32x4 e;
#pragma unroll
        for (int r = 0; r < 4; ++r) { e[r] = __builtin_amdgcn_fmed3f(__builtin_amdgcn_exp2f(z[r]), 0.f, 1.f); p[r] = fmaxf(y[r], 1e-6f); }
        p += e;
        if (t < 3) *(u32x2*)(sm + A_PK + t * 4352 + so) = pk4(p);
        else *(u32x2*)(sm + A_Q + w * 1536 + (16 * (t - 4) + m) * 32 + g * 8) = pk4(p);
      }
      if (t >= 2 && t <= 4) {
        u32x2 u = pk4(v[t] * rs);
        if (t == 4 && m == 8) u = (u32x2){0x3f803f80u, 0x3f803f80u};
        *(u32x2*)(sm + A_V + (t - 2) * 4352 + so) = u;
      }
    }
  }
  BAR(); STAMP(KID, 4);
  const rsrc_t grs = mkrs(ws + O_G + (LAST ? GL : 0) + hp * 16 * GP, 16 * GP);
  {
    f32x4 c = Z4;
#pragma unroll
    for (int k = 0; k < 4; ++k)
      c = mfma(ldf(sm + A_PK + pa * 4352 + m * 272 + k * 64 + g * 16), ldf(sm + A_V + pb * 4352 + m * 272 + k * 64 + g * 16), c);
    u32x4 u = ((__builtin_bit_cast(u32x4, c) + 0x26u) & 0xffffff00u) | 0x5au;
    if (valid) __builtin_amdgcn_raw_buffer_store_b128(u, grs, slot + rb * GP, 0, 16);
  }
  FENCE();
  u32x4 r[16];
  u32x2 x3v[3];
  const int sf = __shfl(slot, __ffsll((unsigned long long)__ballot(valid)) - 1), sl = valid ? slot : sf;
  if (!LAST) {
    WAITV(0);
#pragma unroll
    for (int p = 0; p < 16; ++p) r[p] = __builtin_amdgcn_raw_buffer_load_b128(grs, sl, p * GP, 16);
  }
  if (LAST) {
    WAITV(1); BAR();
    f32x4 y[2][3] = {};
#pragma unroll
    for (int i = 0; i < 9; ++i)
#pragma unroll
      for (int t = 0; t < 3; ++t) {
        bf16x8 qf = ldf(sm + A_QT + (t * 18 + 2 * i + kh) * 1024 + l16);
        y[0][t] = mfma(qf, xf[0][i], y[0][t]); y[1][t] = mfma(qf, xf[1][i], y[1][t]);
      }
    SB();
#pragma unroll
    for (int p = 0; p < 16; ++p) r[p] = __builtin_amdgcn_raw_buffer_load_b128(grs, sl, p * GP, 0);
    SB();
    char* ex = sm + A_PK;
#pragma unroll
    for (int t = 0; t < 3; ++t) *(f32x4*)(ex + (w * 3 + t) * 1024 + l16) = y[1][t];
    BAR();
#pragma unroll
    for (int t = 0; t < 3; ++t) {
      x3v[t] = pk4(y[0][t] + *(const f32x4*)(ex + ((w ^ 4) * 3 + t) * 1024 + l16));
      PIN(x3v[t]);
    }
  }
  FENCE();
  {
    for (int spin = 0; spin < 1000000; ++spin) {
      bool bad = false;
#pragma unroll
      for (int p = 0; p < 16; ++p)
        if (!__all(((r[p][0] & r[p][1]) & (r[p][2] & r[p][3]) & 0xffu) == 0x5au)) {
          FENCE();
          r[p] = __builtin_amdgcn_raw_buffer_load_b128(grs, sl, p * GP, 16);
          bad = true;
        }
      if (!bad) break;
    }
    if (valid) {
      f32x4 sum = Z4;
#pragma unroll
      for (int p = 0; p < 16; ++p) sum += __builtin_bit_cast(f32x4, r[p]);
      u32x2 u = pk4(sum);
      if (isz ? dq <= 4 : !eh) *(u32x2*)(sm + A_KV + (isz ? 18 : ec) * 64 + 16 + 8 * dq) = (u32x2){u[0], dq == 4 ? 0u : u[1]};
      if (isz ? dq >= 4 : eh) *(u32x2*)(sm + A_KV + (isz ? 50 : ec + 14) * 64 - 16 + 8 * dq) = (u32x2){dq == 4 ? 0u : u[0], u[1]};
    }
  }
  if (LAST) {
#pragma unroll
    for (int t = 0; t < 3; ++t)
      if (t < 2 || g == 0) { const int n = 36 * hp + 16 * t + 4 * g; Sp2{ws + O_X3 + ((n >> 3) * S + s0 + 16 * kh + m) * 16 + (n & 4) * 2} = x3v[t]; }
  }
  BAR(); STAMP(KID, 7);
  {
    unsigned tail[2];
    char* ob = ws + (LAST ? O_OB2 : O_OB) + (s0 + 16 * kh + m) * 16;
#pragma unroll
    for (int h = 0; h < 2; ++h) {
      bf16x8 k0 = ldf(sm + A_KV + (32 * h + m) * 64 + g * 16), k1 = ldf(sm + A_KV + (32 * h + 16 + m) * 64 + g * 16);
      bf16x8 pf = trf(sm + A_Q + w * 1536 + (16 * h + 8 * g + (m >> 2)) * 32 + (m & 3) * 8, 128);
      f32x4 n0 = mfma(k0, pf, Z4), n1 = mfma(k1, pf, Z4);
      float inv = __builtin_amdgcn_rcpf(__shfl(n1[2], m) + 1e-6f);
      int P = 9 * hp + 4 * h + g;
      Sp2{ob + (P >> 1) * (S * 16) + (P & 1) * 8} = pk4(n0 * inv);
      tail[h] = pk2(n1[0] * inv, n1[1] * inv);
    }
    int P = 9 * hp + 8;
    if (g == 0) Sp2{ob + (P >> 1) * (S * 16) + (P & 1) * 8} = (u32x2){tail[0], tail[1]};
  }
  FENCE(); STAMP(KID, 8);
}

__global__ void __launch_bounds__(512, 2) k_out(char* ws, char* outp, long layer) {
  const int MODE = 2 + (int)layer;
  const char* obp = ws + (MODE == 2 ? O_OB : O_OB2);
  extern __shared__ __attribute__((aligned(16))) char sm[];
  const int tid = threadIdx.x, w = __builtin_amdgcn_readfirstlane(tid >> 6), l = tid & 63, g = l >> 4, m = l & 15;
  const int b = blockIdx.x, rb = 8 * ((b & 7) >> 1) + ((b >> 3) & 7), ng = 3 * (b & 1) + (b >> 6);
  const int wr = w & 1, ch = (w >> 1) & 1, kh = w >> 2, s = 64 * rb + 32 * wr + 16 * kh + m;
  const int KID = MODE == 2 ? 3 : 6; (void)KID;
  STAMP(KID, 0);
  const rsrc_t wrs = mkrs(ws + (MODE == 2 ? O_WOQ : O_WOF) + ng * 110592, 110592);
  const int l16 = l * 16, xo0 = (g * S + s) * 16, xo1 = xo0 ^ 256;
  bf16x8 xf[2][9];
  auto issue1 = [&](int st, int p) {
    if (p < 3) {
      if (st == 4 && p == 2) return;
      int q = w + 8 * p; if (st == 4) q = q < 12 ? q : q - 8;
      int pc = st < 4 ? (q >> 2) * 18 + 4 * st + (q & 3) : (q >> 1) * 18 + 16 + (q & 1);
      bglds(wrs, sm + pc * 1024, l16, pc * 1024);
    } else {
      const int i = 2 * st + ((p - 3) >> 1);
      if (i > 8) return;
      const char* xb = obp + (2 * i + kh) * (4 * S * 16);
      if (p & 1) ALD16(xf[0][i], xo0, xb, 0); else ALD16(xf[1][i], xo1, xb, 0);
    }
    FENCE(); SB();
  };
  auto issue = [&](int st) {
#pragma unroll
    for (int p = 0; p < 7; ++p) issue1(st, p);
  };
  issue(0); issue(1);
  u32x2 rv[3];
  int ro[3];
#pragma unroll
  for (int t = 0; t < 3; ++t) {
    int n = 16 * (6 * ng + 3 * ch + t) + 4 * g;
    ro[t] = ((n >> 3) * S + s) * 16 + (n & 4) * 2;
  }
  STAMP(KID, 1);
  f32x4 acc[6] = {};
  bf16x8 wf[2][6];
  const char* wa = sm + (54 * ch + kh) * 1024 + l16;
  WAITV(7); BAR(); STAMP(KID, 2);
#pragma unroll
  for (int t = 0; t < 6; ++t) wf[0][t] = ldf(wa + ((t % 3) * 18 + 2 * (t / 3)) * 1024);
  issue(2);
#pragma unroll
  for (int st = 0; st < 5; ++st) {
    if (st < 4) {
      if (st < 3) WAITV(7); else WAITV(3);
      BAR();
    }
#pragma unroll
    for (int k = 0; k < (st < 4 ? 2 : 1); ++k) {
      PIN(xf[0][2 * st + k]); PIN(xf[1][2 * st + k]);
#pragma unroll
      for (int t = 0; t < 3; ++t) {
        acc[t] = mfma(wf[st & 1][3 * k + t], xf[0][2 * st + k], acc[t]); acc[3 + t] = mfma(wf[st & 1][3 * k + t], xf[1][2 * st + k], acc[3 + t]);
        SB();
        if (st < 3 || (st == 3 && k == 0)) wf[(st + 1) & 1][3 * k + t] = ldf(wa + (t * 18 + 4 * st + 4 + 2 * k) * 1024);
        SB();
        if (st < 2) { issue1(st + 3, 3 * k + t); if (k == 1 && t == 2) issue1(st + 3, 6); }
        if (st == 1 && k == 1 && t == 2) {
          const char* rp = ws + (MODE == 2 ? O_XH1 : O_X3);
          ALD8(rv[0], ro[0], rp); ALD8(rv[1], ro[1], rp); ALD8(rv[2], ro[2], rp);
        }
      }
    }
  }
  BAR(); STAMP(KID, 4);
#pragma unroll
  for (int t = 0; t < 3; ++t) *(f32x4*)(sm + (w * 3 + t) * 1024 + l16) = acc[3 + t];
  WAITV(0); BAR();
  char* op = MODE == 2 ? ws + O_XH2 : outp;
  float sq = 0.f;
#pragma unroll
  for (int t = 0; t < 3; ++t) {
    PIN(rv[t]);
    f32x4 v = acc[t] + *(const f32x4*)(sm + ((w ^ 4) * 3 + t) * 1024 + l16);
    v[0] += lo16(rv[t][0]); v[1] += hi16(rv[t][0]); v[2] += lo16(rv[t][1]); v[3] += hi16(rv[t][1]);
    if (MODE == 2) St2{op + ro[t]} = pk4(v); else Sp2{op + s * 1152 + (16 * (6 * ng + 3 * ch + t) + 4 * g) * 2} = pk4(v);
    if (MODE == 2) sq += v[0] * v[0] + v[1] * v[1] + v[2] * v[2] + ((t == 2 && ng == 5 && ch && g == 3) ? 0.f : v[3] * v[3]);
  }
  if (MODE == 2) {
    sq += __shfl_xor(sq, 16); sq += __shfl_xor(sq, 32);
    float* red = (float*)(sm + 32768);
    if (ch && g == 0) red[(2 * wr + kh) * 16 + m] = sq;
    BAR();
    if (!ch && g == 0) ((float*)(ws + O_SS2))[ng * S + s] = sq + red[(2 * wr + kh) * 16 + m];
  }
  FENCE(); STAMP(KID, 5);
}

__global__ void __launch_bounds__(768, 6) k_pre(const char* x0, const char* wo, const char* wr, const char* wi, const char* wqkv, char* ws) {
  extern __shared__ __attribute__((aligned(16))) char sm[];
  const int b = blockIdx.x, tid = threadIdx.x;
  if (b < 192) {
    int x = b & 7, i = b >> 3;
    gemm_q<0>(sm, 4 * x + (i & 3), i >> 2, x0, ws + O_XH0, (float*)(ws + O_SS0));
  } else if (b < 246) {
    int u = b - 192;
    gemm_q<1>(sm, u % 9, u / 9, wo, ws + O_WOQ, nullptr);
  } else {
    STAMP(0, 0);
    int u = b - 246, tg = u >> 3, kb = 8 * (u & 7);
    u32x4 v[4], code;
    u32x2 wpf;
    {
      const char* pc; int len;
      asm volatile("s_getpc_b64 %0\n.Lpre_pc:\n\ts_mov_b32 %1, .Lpre_pc-k_fft1" : "=s"(pc), "=s"(len));
      int off = tg * 12288 + tid * 16;
      off = off + 16 <= len ? off : 0;
      const char* base = len > 0 ? pc - len : pc;
      ALD16(code, off, base, 0);
      const int wq = (u * 243 + (tid < 243 ? tid : 0)) * 128;
      ALD8(wpf, wq, wqkv);
    }
#pragma unroll
    for (int i = 0; i < 4; ++i) {
      int e = i * 768 + tid; e = e < 2312 ? e : 2311;
      int run = e / 289, o = e - 289 * run;
      v[i] = *(const u32x4*)((run < 4 ? wr : wi) + (kb + 64 * (4 * tg + (run & 3))) * 578 + o * 16);
    }
    {
      const int id = u * 768 + tid;
      St4{ws + O_G + id * 16} = (u32x4){0u, 0u, 0u, 0u};
      if (id < 4608) St4{ws + O_WF + ((7 * (id / 288) + 6) * 18 + (id % 288 >> 4)) * 1024 + (12 + (id & 3) + 16 * ((id >> 2) & 3)) * 16} = (u32x4){0u, 0u, 0u, 0u};
    }
#pragma unroll
    for (int i = 0; i < 4; ++i) { int e = i * 768 + tid; e = e < 2312 ? e : 2311; *(u32x4*)(sm + e * 16) = v[i]; }
    __syncthreads();
    STAMP(0, 2);
#pragma unroll
    for (int i = 0; i < 3; ++i) {
      int e = i * 768 + tid, mm = e & 7, c = e >> 3;
      const char* p = sm + mm * 578 + 2 * (c + 1);
      u32x4 o;
#pragma unroll
      for (int r = 0; r < 4; ++r) o[r] = (unsigned)*(const u16*)(p + r * 4624) | ((unsigned)*(const u16*)(p + (4 + r) * 4624) << 16);
      St4{ws + O_WT + ((c * 4 + (kb >> 4)) * 2 + (tg >> 2)) * 1024 + (16 * (tg & 3) + (kb & 8) + mm) * 16} = o;
    }
    WAITV(0); PIN(code); PIN(wpf);
    FENCE(); STAMP(0, 5);
  }
}

extern "C" void kernel_launch(void* const* d_in, const int* in_sizes, int n_in, void* d_out, int out_size, void* d_ws,
                              size_t ws_size, hipStream_t stream) {
  const char *x0 = (const char*)d_in[0], *wr = (const char*)d_in[1], *wi = (const char*)d_in[2];
  const char *wqkv = (const char*)d_in[4], *wo = (const char*)d_in[5];
  char* ws = (char*)d_ws;
  (void)hipFuncSetAttribute((const void*)k_pre, hipFuncAttributeMaxDynamicSharedMemorySize, 73728);
  (void)hipFuncSetAttribute((const void*)k_fft1, hipFuncAttributeMaxDynamicSharedMemorySize, 73728);
  (void)hipFuncSetAttribute((const void*)k_attn, hipFuncAttributeMaxDynamicSharedMemorySize, 163328);
  (void)hipFuncSetAttribute((const void*)k_out, hipFuncAttributeMaxDynamicSharedMemorySize, 110592);
  k_pre<<<310, 768, 73728, stream>>>(x0, wo, wr, wi, wqkv, ws);
  k_fft1<<<252, 512, 73728, stream>>>(wqkv, ws, 0L);
  k_attn<<<256, 512, 163328, stream>>>(ws, 0L);
  k_out<<<192, 512, 110592, stream>>>(ws, (char*)d_out, 0L);
  k_fft1<<<252, 512, 40960, stream>>>(wo, ws, 1L);
  k_attn<<<256, 512, 163328, stream>>>(ws, 1L);
  k_out<<<192, 512, 110592, stream>>>(ws, (char*)d_out, 1L);
}
```

```cpp
#include <hip/hip_runtime.h>
#include <hip/hip_bf16.h>

typedef __bf16 bf16x8 __attribute__((ext_vector_type(8)));
typedef __bf16 bf16x2 __attribute__((ext_vector_type(2)));
typedef short s16x4 __attribute__((ext_vector_type(4)));
typedef float f32x4 __attribute__((ext_vector_type(4)));
typedef float f32x2 __attribute__((ext_vector_type(2)));
typedef unsigned u32x2 __attribute__((ext_vector_type(2)));
typedef unsigned u32x4 __attribute__((ext_vector_type(4)));
typedef unsigned short u16;
#define LDSP __attribute__((address_space(3)))
#define GLBP __attribute__((address_space(1)))
#define DI __device__ __forceinline__
#define FENCE() asm volatile("" ::: "memory")
#define WAITV(n) asm volatile("s_waitcnt vmcnt(" #n ")" ::: "memory")
#define SB() __builtin_amdgcn_sched_barrier(0)
#define BAR() do { SB(); asm volatile("s_waitcnt lgkmcnt(0)" ::: "memory"); __builtin_amdgcn_s_barrier(); FENCE(); SB(); } while (0)

constexpr int S = 2048, D = 576;
constexpr bool ALIAS = false;
constexpr size_t XB = (size_t)S * D * 2;
constexpr size_t O_XH0 = 0, O_XH1 = XB, O_XH2 = ALIAS ? 0 : 2 * XB, O_XH3 = ALIAS ? XB : 3 * XB, O_OB = (ALIAS ? 2 : 4) * XB;
constexpr size_t O_WT = (ALIAS ? 3 : 6) * XB, O_X3 = ALIAS ? O_WT : 5 * XB;
constexpr size_t O_WOQ = ALIAS ? 4 * XB : 7 * XB + 663552, O_WOF = O_WOQ + 663552, O_WF = O_WOF + 663552;
constexpr size_t O_QTF = O_WF + 2064384, O_SS0 = O_QTF + 884736, O_SS2 = O_SS0 + 8192, O_G = O_SS2 + 6 * 8192;
constexpr int GP = 3072;
constexpr size_t GL = (size_t)256 * GP;
constexpr size_t O_OB2 = O_G + 2 * GL;

DI f32x4 mfma(bf16x8 a, bf16x8 b, f32x4 c) { return __builtin_amdgcn_mfma_f32_16x16x32_bf16(a, b, c, 0, 0, 0); }
DI unsigned pk2(float a, float b) { f32x2 f = {a, b}; return __builtin_bit_cast(unsigned, __builtin_convertvector(f, bf16x2)); }
DI bf16x8 pk8(f32x4 a, f32x4 b) { u32x4 u = {pk2(a[0], a[1]), pk2(a[2], a[3]), pk2(b[0], b[1]), pk2(b[2], b[3])}; return __builtin_bit_cast(bf16x8, u); }
DI float lo16(unsigned u) { return __builtin_bit_cast(float, u << 16); }
DI float hi16(unsigned u) { return __builtin_bit_cast(float, u & 0xffff0000u); }
DI float cosr(float rev) { return __builtin_amdgcn_cosf(__builtin_amdgcn_fractf(rev)); }
DI float sinr(float rev) { return __builtin_amdgcn_sinf(__builtin_amdgcn_fractf(rev)); }
DI void glds(const char* g, char* l) { __builtin_amdgcn_global_load_lds((const GLBP void*)g, (LDSP void*)l, 16, 0, 0); }
DI bf16x8 ldf(const char* p) { return *(const bf16x8*)p; }
typedef __amdgpu_buffer_rsrc_t rsrc_t;
DI rsrc_t mkrs(const void* p, unsigned bytes) { return __builtin_amdgcn_make_buffer_rsrc((void*)p, 0, bytes, 0x00020000); }
DI void bglds(rsrc_t r, char* l, int voff, int soff) { __builtin_amdgcn_raw_ptr_buffer_load_lds(r, (LDSP void*)l, 16, voff, soff, 0, 0); }
#define ALD16(dst, voff, sbase, imm) asm volatile("global_load_dwordx4 %0, %1, %2 offset:" #imm : "=v"(dst) : "v"(voff), "s"(sbase))
#define ALD8(dst, voff, sbase) asm volatile("global_load_dwordx2 %0, %1, %2" : "=v"(dst) : "v"(voff), "s"(sbase))
#define PIN(x) asm volatile("" : "+v"(x))
DI bf16x8 trf(const char* p, int d) {
  s16x4 lo = __builtin_amdgcn_ds_read_tr16_b64_v4i16((LDSP s16x4*)p), hi = __builtin_amdgcn_ds_read_tr16_b64_v4i16((LDSP s16x4*)(p + d));
  return __builtin_bit_cast(bf16x8, __builtin_shufflevector(lo, hi, 0, 1, 2, 3, 4, 5, 6, 7));
}
struct St4 { char* p; DI void operator=(u32x4 v) const { asm volatile("global_store_dwordx4 %0, %1, off sc1\n\ts_nop 1" :: "v"(p), "v"(v) : "memory"); } };
struct St2 { char* p; DI void operator=(u32x2 v) const { asm volatile("global_store_dwordx2 %0, %1, off sc1" :: "v"(p), "v"(v) : "memory"); } };
struct Sp2 { char* p; DI void operator=(u32x2 v) const { *(u32x2*)p = v; } };
DI u32x2 pk4(f32x4 v) { return (u32x2){pk2(v[0], v[1]), pk2(v[2], v[3])}; }
constexpr u32x4 ONES = {0x3f803f80u, 0x3f803f80u, 0x3f803f80u, 0x3f803f80u};
constexpr f32x4 Z4 = {0.f, 0.f, 0.f, 0.f};
DI float qent(int d, int j) {
  int f = (j >> 1) + 1; float ph = (j & 1) ? 0.25f : 0.f, sc = 0.05892556509887896f;
  if (j >= 574) { sc = 0.041666666666666664f; ph = 0.f; f = (j == 575) ? 0 : 288; }
  return sc * cosr((float)(f * d) * (1.f / 576.f) + ph);
}
DI int operm(int k) {
  int hp = k / 36, c = k - 36 * hp, p = c >> 2, i = c & 3;
  return p < 8 ? 18 * (2 * hp + (p >> 2)) + 4 * (p & 3) + i : 18 * (2 * hp + (i >> 1)) + 16 + (i & 1);
}

DI void qgen(bf16x8* qf, int j, int d0, int dd) {
  int f = (j >> 1) + 1; float ph = (j & 1) ? 0.25f : 0.f, sc = 0.05892556509887896f;
  if (j >= 574) { sc = 0.041666666666666664f; ph = 0.f; f = (j == 575) ? 0 : 288; }
  const float st = (float)f * (1.f / 576.f), c2 = 2.f * __builtin_amdgcn_cosf(st), p0 = st * (float)d0 + ph;
  const float cd = 2.f * cosr(st * (float)dd);
  f32x2 u[2][4];
#pragma unroll
  for (int i = 0; i < 9; ++i) {
    if (i < 2) {
      float p = __builtin_amdgcn_fractf(st * (float)(dd * i) + p0), v[8];
      v[0] = sc * __builtin_amdgcn_cosf(p); v[1] = sc * __builtin_amdgcn_cosf(p + st);
#pragma unroll
      for (int e = 2; e < 8; ++e) v[e] = c2 * v[e - 1] - v[e - 2];
#pragma unroll
      for (int e = 0; e < 4; ++e) u[i][e] = (f32x2){v[2 * e], v[2 * e + 1]};
    } else {
#pragma unroll
      for (int e = 0; e < 4; ++e) u[i & 1][e] = __builtin_elementwise_fma((f32x2){cd, cd}, u[(i & 1) ^ 1][e], -u[i & 1][e]);
    }
    const f32x2* x = u[i & 1];
    qf[i] = __builtin_bit_cast(bf16x8, (u32x4){pk2(x[0][0], x[0][1]), pk2(x[1][0], x[1][1]), pk2(x[2][0], x[2][1]), pk2(x[3][0], x[3][1])});
    PIN(qf[i]);
  }
}
template <int MODE>
DI void gemm_q(char* sm, int rb, int ng, const char* xs, char* out, float* ssq) {
  const int tid = threadIdx.x, w = __builtin_amdgcn_readfirstlane(tid >> 6), l = tid & 63, g = l >> 4, m = l & 15;
  const int kh = w >= 6, nt = w - 6 * kh, jt = 6 * ng + nt;
  {
    const rsrc_t xrs = mkrs(xs, MODE == 0 ? (unsigned)XB : 663552u);
    int voff[2];
#pragma unroll
    for (int h = 0; h < 2; ++h) {
      int r = 64 * rb + 8 * ((w + 4 * h) & 7) + (l >> 3);
      voff[h] = (MODE == 1 ? operm(r) : r) * 1152 + (((l & 7) ^ (l >> 3)) << 4);
    }
#pragma unroll
    for (int i = 0; i < 6; ++i) { int q = w + 12 * i; bglds(xrs, sm + q * 1024, voff[i & 1], (q >> 3) * 128); }
  }
  SB();
  bf16x8 qf[9];
  qgen(qf, 16 * jt + m, 8 * g + 32 * kh, 64);
  WAITV(0); BAR();
  f32x4 acc[4] = {}, gr = Z4, su = Z4;
  const bool stats = MODE == 0 && ng == 0 && nt < 4;
  const int x2 = 2 * kh;
  const char* xb = sm + (m >> 3) * 1024 + (m & 7) * 128 + (((4 * kh + g) ^ (m & 7)) << 4);
#pragma unroll
  for (int i = 0; i < 9; ++i) {
#pragma unroll
    for (int a = 0; a < 4; ++a) {
      bf16x8 xf = ldf(xb + i * 8192 + (a ^ x2) * 2048);
      acc[a] = MODE == 1 ? mfma(xf, qf[i], acc[a]) : mfma(qf[i], xf, acc[a]);
    }
    if (stats) { bf16x8 xf = ldf(xb + i * 8192 + nt * 2048); gr = mfma(xf, xf, gr); su = mfma(__builtin_bit_cast(bf16x8, ONES), xf, su); }
  }
  BAR();
  *(f32x4*)(sm + w * 4096 + l * 16) = acc[2]; *(f32x4*)(sm + w * 4096 + 1024 + l * 16) = acc[3];
  if (stats && kh) { *(f32x4*)(sm + w * 4096 + 2048 + l * 16) = gr; *(f32x4*)(sm + w * 4096 + 3072 + l * 16) = su; }
  BAR();
  const char* pp = sm + (kh ? w - 6 : w + 6) * 4096 + l * 16;
#pragma unroll
  for (int a = 0; a < 2; ++a) {
    f32x4 v = acc[a] + *(const f32x4*)(pp + a * 1024);
    const int rt = 4 * rb + x2 + a, n = 16 * jt + 4 * g;
    if (MODE == 1) St2{out + (jt * 18 + (rt >> 1)) * 1024 + (m + 16 * (2 * (rt & 1) + (g >> 1))) * 16 + (g & 1) * 8} = pk4(v);
    else St2{out + ((n >> 3) * S + 16 * rt + m) * 16 + (n & 4) * 2} = pk4(v);
  }
  if (stats && !kh) {
    gr += *(const f32x4*)(pp + 2048); su += *(const f32x4*)(pp + 3072);
    float dsel = (m & 2) ? ((m & 1) ? gr[3] : gr[2]) : ((m & 1) ? gr[1] : gr[0]);
    if ((m >> 2) == g) ssq[64 * rb + 16 * nt + m] = fmaxf(dsel - su[0] * su[0] * (1.f / 576.f), 0.f);
  }
  FENCE();
}

DI void fft_cols(char* sm, int b, const int second, const char* xin, char* xout, const float* ssq, const char* wt) {
  const int tid = threadIdx.x, w8 = __builtin_amdgcn_readfirstlane(tid >> 6), l = tid & 63, g = l >> 4, m = l & 15;
  const int w = w8 & 3, hi = w8 >> 2;
  const int kb = (b & 7) + 8 * (b >> 4), half = (b >> 3) & 1;
  const bool last = kb == 71 && half == 1;
  f32x4 ps[6];
#pragma unroll
  for (int p = 0; p < 6; ++p) ps[p] = *(const f32x4*)(ssq + p * (second ? S : 0) + tid * 4);
  u32x2 xv[4];
  const int xo = (kb * S + 32 * (16 * w + 4 * g) + 16 * hi + m) * 16 + 8 * half;
#pragma unroll
  for (int r = 0; r < 4; ++r) xv[r] = *(const u32x2*)(xin + xo + r * 512);
  u32x4 wv[2];
#pragma unroll
  for (int t = 0; t < 2; ++t) wv[t] = *(const u32x4*)(wt + (((4 * kb + 2 * half + hi) * 4 + w) * 2 + t) * 1024 + l * 16);
  FENCE();
  SB();
  bf16x8 c64[2], s64[2], n64[2], c32[2], s32[2], n32[2];
  f32x4 t1c[2], t1s[2], t2c[2], t2s[2];
  {
    const float k1 = (float)(16 * w + m), g4 = (float)(4 * g);
    const unsigned sg = (m & 1) ? 0x80008000u : 0u;
    float c[8], sn[8];
    const float a = k1 * (1.f / 64.f), a2 = 2.f * __builtin_amdgcn_cosf(a), p = a * (g4 + g4);
    c[0] = __builtin_amdgcn_cosf(p); c[1] = __builtin_amdgcn_cosf(p + a); sn[0] = __builtin_amdgcn_sinf(p); sn[1] = __builtin_amdgcn_sinf(p + a);
#pragma unroll
    for (int j = 2; j < 8; ++j) { c[j] = a2 * c[j - 1] - c[j - 2]; sn[j] = a2 * sn[j - 1] - sn[j - 2]; }
    u32x4 uc = {pk2(c[0], c[1]), pk2(c[2], c[3]), pk2(c[4], c[5]), pk2(c[6], c[7])}, us = {pk2(sn[0], sn[1]), pk2(sn[2], sn[3]), pk2(sn[4], sn[5]), pk2(sn[6], sn[7])};
    c64[0] = __builtin_bit_cast(bf16x8, uc); c64[1] = __builtin_bit_cast(bf16x8, uc ^ sg);
    s64[0] = __builtin_bit_cast(bf16x8, us); s64[1] = __builtin_bit_cast(bf16x8, us ^ sg);
    n64[0] = __builtin_bit_cast(bf16x8, us ^ 0x80008000u); n64[1] = __builtin_bit_cast(bf16x8, us ^ (sg ^ 0x80008000u));
    const float e = (float)m * (1.f / 32.f);
#pragma unroll
    for (int j = 0; j < 4; ++j) { float q = e * (g4 + (float)j); c[j] = __builtin_amdgcn_cosf(q); sn[j] = __builtin_amdgcn_sinf(q); }
    unsigned c0 = pk2(c[0], c[1]), c1 = pk2(c[2], c[3]), s0 = pk2(sn[0], sn[1]), s1 = pk2(sn[2], sn[3]);
    uc = (u32x4){c0, c1, c0 ^ sg, c1 ^ sg}; us = (u32x4){s0, s1, s0 ^ sg, s1 ^ sg};
    c32[0] = __builtin_bit_cast(bf16x8, uc); c32[1] = __builtin_bit_cast(bf16x8, uc ^ 0x80000000u);
    s32[0] = __builtin_bit_cast(bf16x8, us); s32[1] = __builtin_bit_cast(bf16x8, us ^ 0x80000000u);
    n32[0] = __builtin_bit_cast(bf16x8, us ^ 0x80008000u); n32[1] = __builtin_bit_cast(bf16x8, us ^ 0x00008000u);
    const float b1 = k1 * (1.f / 2048.f), b12 = 2.f * __builtin_amdgcn_cosf(b1);
#pragma unroll
    for (int h = 0; h < 2; ++h) {
      const float p1 = b1 * (g4 + (float)(16 * h)), b2 = (float)(16 * h + m) * (1.f / 2048.f), b22 = 2.f * __builtin_amdgcn_cosf(b2), p2 = b2 * (g4 + (float)(16 * w));
      t1c[h][0] = __builtin_amdgcn_cosf(p1); t1c[h][1] = __builtin_amdgcn_cosf(p1 + b1); t1s[h][0] = __builtin_amdgcn_sinf(p1); t1s[h][1] = __builtin_amdgcn_sinf(p1 + b1);
      t2c[h][0] = __builtin_amdgcn_cosf(p2); t2c[h][1] = __builtin_amdgcn_cosf(p2 + b2); t2s[h][0] = __builtin_amdgcn_sinf(p2); t2s[h][1] = __builtin_amdgcn_sinf(p2 + b2);
#pragma unroll
      for (int r = 2; r < 4; ++r) {
        t1c[h][r] = b12 * t1c[h][r - 1] - t1c[h][r - 2]; t1s[h][r] = b12 * t1s[h][r - 1] - t1s[h][r - 2];
        t2c[h][r] = b22 * t2c[h][r - 1] - t2c[h][r - 2]; t2s[h][r] = b22 * t2s[h][r - 1] - t2s[h][r - 2];
      }
      PIN(c64[h]); PIN(s64[h]); PIN(n64[h]); PIN(c32[h]); PIN(s32[h]); PIN(n32[h]); PIN(t1c[h]); PIN(t1s[h]); PIN(t2c[h]); PIN(t2s[h]);
    }
  }
  SB();
  FENCE();
  float* rsl = (float*)(sm + 32768);
  {
    f32x4 a = ps[0];
#pragma unroll
    for (int p = 1; p < 6; ++p) a += ps[p] * (second ? 1.f : 0.f);
#pragma unroll
    for (int i = 0; i < 4; ++i) a[i] = __builtin_amdgcn_rsqf(a[i] * (1.f / 576.f) + 1e-5f) * (1.f / 2048.f);
    *(f32x4*)(rsl + tid * 4) = a;
  }
  BAR();
  const int wo = m * 128 + (((2 * w + (g >> 1)) ^ (m & 7)) << 4) + (g & 1) * 8;
  {
    f32x4 u[4];
#pragma unroll
    for (int r = 0; r < 4; ++r) {
      float rs = rsl[32 * (16 * w + 4 * g + r) + 16 * hi + m];
      u[0][r] = lo16(xv[r][0]) * rs; u[1][r] = hi16(xv[r][0]) * rs; u[2][r] = lo16(xv[r][1]) * rs;
      u[3][r] = last ? 0.f : hi16(xv[r][1]) * rs;
    }
#pragma unroll
    for (int p = 0; p < 4; ++p) *(u32x2*)(sm + p * 4096 + hi * 2048 + wo) = pk4(u[p]);
  }
  BAR();
  const int ro0 = m * 128 + ((g ^ (m & 7)) << 4), ro1 = ro0 ^ 64;
  {
    f32x4 re[2], im[2];
#pragma unroll
    for (int t = 0; t < 2; ++t) {
      const char* p = sm + hi * 8192 + t * 2048;
      bf16x8 a0 = ldf(p + ro0), a1 = ldf(p + ro1), b0 = ldf(p + 4096 + ro0), b1 = ldf(p + 4096 + ro1);
      f32x4 x = mfma(a0, c64[0], Z4), y = mfma(b0, c64[0], Z4);
      x = mfma(a1, c64[1], x); y = mfma(b1, c64[1], y);
      x = mfma(b0, s64[0], x); y = mfma(a0, n64[0], y);
      x = mfma(b1, s64[1], x); y = mfma(a1, n64[1], y);
#pragma unroll
      for (int r = 0; r < 4; ++r) { re[t][r] = x[r] * t1c[t][r] + y[r] * t1s[t][r]; im[t][r] = y[r] * t1c[t][r] - x[r] * t1s[t][r]; }
    }
    bf16x8 fr = pk8(re[0], re[1]), fi = pk8(im[0], im[1]);
#pragma unroll
    for (int t = 0; t < 2; ++t) {
      f32x4 x = mfma(c32[t], fr, Z4), y = mfma(c32[t], fi, Z4);
      x = mfma(s32[t], fi, x); y = mfma(n32[t], fr, y);
#pragma unroll
      for (int r = 0; r < 4; ++r) {
        float a = lo16(wv[t][r]), bb = hi16(wv[t][r]);
        re[t][r] = x[r] * a - y[r] * bb; im[t][r] = x[r] * bb + y[r] * a;
      }
    }
    fr = pk8(re[0], re[1]); fi = pk8(im[0], im[1]);
#pragma unroll
    for (int t = 0; t < 2; ++t) {
      f32x4 x = mfma(fr, c32[t], Z4), y = mfma(fi, c32[t], Z4);
      x = mfma(fi, n32[t], x); y = mfma(fr, s32[t], y);
      f32x4 vr, vi;
#pragma unroll
      for (int r = 0; r < 4; ++r) { vr[r] = x[r] * t2c[t][r] - y[r] * t2s[t][r]; vi[r] = y[r] * t2c[t][r] + x[r] * t2s[t][r]; }
      char* p = sm + 16384 + hi * 8192 + t * 2048 + wo;
      *(u32x2*)p = pk4(vr); *(u32x2*)(p + 4096) = pk4(vi);
    }
  }
  BAR();
  {
    f32x4 yr[2], yi[2];
#pragma unroll
    for (int c = 0; c < 2; ++c) {
      const char* p = sm + 16384 + c * 8192 + hi * 2048;
      bf16x8 a0 = ldf(p + ro0), a1 = ldf(p + ro1), b0 = ldf(p + 4096 + ro0), b1 = ldf(p + 4096 + ro1);
      f32x4 x = mfma(c64[0], a0, Z4), y = mfma(c64[0], b0, Z4);
      x = mfma(c64[1], a1, x); y = mfma(c64[1], b1, y);
      x = mfma(n64[0], b0, x); y = mfma(s64[0], a0, y);
      x = mfma(n64[1], b1, x); y = mfma(s64[1], a1, y);
      yr[c] = x; yi[c] = y;
    }
#pragma unroll
    for (int r = 0; r < 4; ++r) {
      u32x2 o = {pk2(lo16(xv[r][0]) + yr[0][r], hi16(xv[r][0]) + yi[0][r]),
                 pk2(lo16(xv[r][1]) + yr[1][r], hi16(xv[r][1]) + (last ? 0.f : yi[1][r]))};
      Sp2{xout + xo + r * 512} = o;
    }
  }
  FENCE();
}

DI void wqkv_rot(char* sm, int u, const char* wqkv, char* wf) {
  const int tid = threadIdx.x, w = __builtin_amdgcn_readfirstlane(tid >> 6), l = tid & 63, g = l >> 4, m = l & 15;
  const int jg = u & 3, ng = u >> 2, kh = w >> 2, wc = w & 3, jt = 9 * jg + 2 * wc + kh, j8 = 9 * jg + 8;
  const rsrc_t rs = mkrs(wqkv, 1990656u);
  const int voff = (l >> 3) * 3456 + (((((l >> 1) & 3) ^ (((l >> 4) & 1) | ((w & 1) << 1))) << 5) | ((l & 1) << 4));
  const int l16 = l * 16;
#pragma unroll
  for (int i = 0; i < 9; ++i) bglds(rs, sm + (w + 8 * i) * 1024, voff, (w + 8 * i) * 27648 + ng * 128);
  SB();
  bf16x8 af[3][9];
  qgen(af[0], 16 * jt + m, 288 * kh + 8 * g, 32);
  qgen(af[1], 16 * (jt + 1 - 2 * kh) + m, 288 * kh + 8 * g, 32);
  qgen(af[2], 16 * j8 + m, 288 * kh + 8 * g, 32);
  int ob[4];
#pragma unroll
  for (int t = 0; t < 4; ++t) {
    int n = 64 * ng + 16 * (t ^ wc) + m, seg = (n >= 576) + (n >= 1152), c = n - 576 * seg, hp = c / 36;
    int cc = c - 36 * hp + (seg == 0 ? 72 : seg == 1 ? 0 : 36);
    ob[t] = (7 * hp + (cc >> 4)) * 18432 + (cc & 15) * 16;
  }
  WAITV(0); BAR();
  f32x4 acc[2][4] = {}, a8 = Z4;
  const int rbase = 9 * kh * 4096 + (8 * g + (m >> 2)) * 128 + 8 * (m & 3), fx = (((m >> 3) & 1) | ((g & 1) << 1)) ^ wc;
#pragma unroll
  for (int i = 0; i < 9; ++i) {
#pragma unroll
    for (int t = 0; t < 4; ++t) {
      bf16x8 bfr = trf(sm + i * 4096 + rbase + ((t ^ fx) << 5), 512);
      acc[0][t] = mfma(af[0][i], bfr, acc[0][t]); acc[1][t] = mfma(af[1][i], bfr, acc[1][t]);
      if (t == 0) a8 = mfma(af[2][i], bfr, a8);
    }
  }
  BAR();
#pragma unroll
  for (int t = 0; t < 4; ++t) *(f32x4*)(sm + (w * 5 + t) * 1024 + l16) = acc[1][t];
  *(f32x4*)(sm + (w * 5 + 4) * 1024 + l16) = a8;
  BAR();
  const int jo = (jt >> 1) * 1024 + (2 * (jt & 1) + (g >> 1)) * 256 + (g & 1) * 8;
#pragma unroll
  for (int t = 0; t < 4; ++t) St2{wf + ob[t] + jo} = pk4(acc[0][t] + *(const f32x4*)(sm + ((w ^ 4) * 5 + t) * 1024 + l16));
  if (!kh) {
    f32x4 o = a8 + *(const f32x4*)(sm + ((w ^ 4) * 5 + 4) * 1024 + l16);
    if (j8 == 35 && g == 3) o[3] = 0.f;
    St2{wf + ob[0] + (j8 >> 1) * 1024 + (2 * (j8 & 1) + (g >> 1)) * 256 + (g & 1) * 8} = pk4(o);
  }
  FENCE();
}

extern "C" __global__ void __launch_bounds__(512, 2) k_fft1(const char* wsrc, char* ws, long layer) {
  extern __shared__ __attribute__((aligned(16))) char sm[];
  const int b = blockIdx.x, second = (int)layer;
  if (b < 144) fft_cols(sm, b, second, ws + (second ? O_XH2 : O_XH0), ws + (second ? O_XH3 : O_XH1), (const float*)(ws + (second ? O_SS2 : O_SS0)), ws + O_WT);
  else if (!second) wqkv_rot(sm, b - 144, wsrc, ws + O_WF);
  else {
    const char* wo = wsrc;
    const int e = (b - 144) * 512 + threadIdx.x, l = e & 63, pc = e >> 6, nt = pc / 18, ks = pc - 18 * nt;
    unsigned h[8];
    if (e < 41472) {
      const int r0 = operm(32 * ks + 8 * (l >> 4)), r1 = operm(32 * ks + 8 * (l >> 4) + 4), c0 = (32 * ks + 8 * (l >> 4)) % 36;
#pragma unroll
      for (int j = 0; j < 8; ++j) {
        int r = (j < 4 ? r0 : r1) + (c0 == (j < 4 ? 32 : 28) ? 18 * ((j >> 1) & 1) + (j & 1) : (j & 3));
        h[j] = *(const u16*)(wo + r * 1152 + (16 * nt + (l & 15)) * 2);
      }
    }
    if (e < (int)(GL / 16)) St4{ws + O_G + GL + e * 16} = (u32x4){0u, 0u, 0u, 0u};
    const int hp = nt / 3, c = 16 * (nt - 3 * hp) + (l & 15), d = 36 * hp + (c < 36 ? c : 0), j0 = 32 * ks + 8 * (l >> 4);
    St4{ws + O_QTF + e * 16} = (u32x4){pk2(qent(d, j0), qent(d, j0 + 1)), pk2(qent(d, j0 + 2), qent(d, j0 + 3)),
                                             pk2(qent(d, j0 + 4), qent(d, j0 + 5)), pk2(qent(d, j0 + 6), qent(d, j0 + 7))};
    if (e < 41472) St4{ws + O_WOF + e * 16} = (u32x4){h[0] | (h[1] << 16), h[2] | (h[3] << 16), h[4] | (h[5] << 16), h[6] | (h[7] << 16)};
  }
}

constexpr int A_QT = 103936, A_KV = A_QT + 55296;
constexpr int A_PK = 65536, A_V = A_PK + 13056, A_Q = A_V + 13056;
__global__ void __launch_bounds__(512, 2) k_attn(char* ws, long layer) {
  const int LAST = (int)layer;
  const char* xin = ws + (LAST ? O_XH3 : O_XH1);
  extern __shared__ __attribute__((aligned(16))) char sm[];
  const int tid = threadIdx.x, w = __builtin_amdgcn_readfirstlane(tid >> 6), l = tid & 63, g = l >> 4, m = l & 15;
  const int b = blockIdx.x, rb = 4 * ((b & 7) >> 1) + ((b >> 3) & 3), hp = 8 * (b & 1) + (b >> 5);
  const int wr = w & 3, kh = w >> 2, s0 = 128 * rb + 32 * wr;
  const rsrc_t wrs = mkrs(ws + O_WF + hp * 129024, 129024);
  const int l16 = l * 16, xo0 = (g * S + s0 + 16 * kh + m) * 16, xo1 = xo0 ^ 256;
  bf16x8 xf[2][9];
  auto issue1 = [&](int j, int p) {
    if (p < 2) {
      int q = w + 8 * p; q = q < 14 ? q : q - 8;
      int pc = (q >> 1) * 18 + 2 * j + (q & 1);
      bglds(wrs, sm + pc * 1024, l16, pc * 1024);
    } else {
      const char* xb = xin + (2 * j + kh) * (4 * S * 16);
      if (p == 2) ALD16(xf[0][j], xo0, xb, 0); else ALD16(xf[1][j], xo1, xb, 0);
    }
    FENCE(); SB();
  };
  auto issue = [&](int j) { issue1(j, 0); issue1(j, 1); issue1(j, 2); issue1(j, 3); };
#pragma unroll
  for (int j = 0; j < 3; ++j) issue(j);
  if (tid < 256) *(u32x4*)(sm + A_KV + tid * 16) = (u32x4){0u, 0u, 0u, 0u};
  const int pa = w < 6 ? w / 3 : 2, pb = w < 6 ? w - 3 * pa : w - 5;
  const int dq = 4 * pa + g, ec = 16 * pb + m - 4, eh = ec >= 18;
  const bool isz = ec == 36, valid = isz ? dq < 9 : (ec >= 0 && ec < 36 && dq >= 4 * eh && dq <= 4 * eh + 4);
  const int slot = (isz ? 180 + dq : 36 * (dq - 4 * eh) + ec) * 16;
  f32x4 acc[2][7] = {};
  float sq[2] = {0.f, 0.f};
  bf16x8 wf[2][7];
  WAITV(8); BAR();
#pragma unroll
  for (int t = 0; t < 7; ++t) wf[0][t] = ldf(sm + (t * 18 + kh) * 1024 + l16);
  issue(3);
#pragma unroll
  for (int i = 0; i < 9; ++i) {
    if (i < 8) {
      if (i <= 5) WAITV(8); else if (i == 6) WAITV(4); else WAITV(0);
      BAR();
    }
    PIN(xf[0][i]); PIN(xf[1][i]);
#pragma unroll
    for (int t = 0; t < 7; ++t) {
      acc[0][t] = mfma(xf[0][i], wf[i & 1][t], acc[0][t]); acc[1][t] = mfma(xf[1][i], wf[i & 1][t], acc[1][t]);
      if (t < 4) {
#pragma unroll
        for (int a = 0; a < 2; ++a) {
          unsigned u = __builtin_bit_cast(u32x4, xf[a][i])[t];
          if (i == 8 && t == 3) u = (g == 3 && kh) ? (u & 0xffffu) : u;
          bf16x2 h = __builtin_bit_cast(bf16x2, u);
          sq[a] = __builtin_amdgcn_fdot2_f32_bf16(h, h, sq[a], false);
        }
      }
      SB();
      if (i < 8) wf[(i + 1) & 1][t] = ldf(sm + (t * 18 + 2 * i + 2 + kh) * 1024 + l16);
      SB();
      if (!(t & 1) && i + 4 < 9) issue1(i + 4, t >> 1);
    }
  }
  BAR();
  if (LAST) {
    const rsrc_t qrs = mkrs(ws + O_QTF + hp * 55296, 55296);
#pragma unroll
    for (int i = 0; i < 7; ++i) { int q = w + 8 * i; q = q < 54 ? q : q - 8; bglds(qrs, sm + A_QT + q * 1024, l16, q * 1024); }
  }
  SB();
#pragma unroll
  for (int t = 0; t < 7; ++t) *(f32x4*)(sm + (w * 8 + t) * 1024 + l16) = acc[1][t];
  *(float*)(sm + (w * 8 + 7) * 1024 + l * 4) = sq[1];
  BAR();
  float rq = sq[0] + *(const float*)(sm + ((w ^ 4) * 8 + 7) * 1024 + l * 4);
  f32x4 v[7];
#pragma unroll
  for (int t = 0; t < 7; ++t) v[t] = acc[0][t] + *(const f32x4*)(sm + ((w ^ 4) * 8 + t) * 1024 + l16);
  rq += __shfl_xor(rq, 16); rq += __shfl_xor(rq, 32);
  rq = __builtin_amdgcn_rsqf(rq * (1.f / 576.f) + 1e-5f);
  {
    f32x4 rs, rl;
#pragma unroll
    for (int r = 0; r < 4; ++r) { rs[r] = __shfl(rq, 4 * g + r); rl[r] = rs[r] * 1.4426950408889634f; }
    const int so = (32 * wr + 16 * kh + 4 * g) * 2 + m * 272;
#pragma unroll
    for (int t = 0; t < 7; ++t) {
      if (t != 3) {
        f32x4 p;
        const f32x4 z = v[t] * rl, y = __builtin_elementwise_fma(v[t], rs, (f32x4){1e-6f, 1e-6f, 1e-6f, 1e-6f});
        f32x4 e;
#pragma unroll
        for (int r = 0; r < 4; ++r) { e[r] = __builtin_amdgcn_fmed3f(__builtin_amdgcn_exp2f(z[r]), 0.f, 1.f); p[r] = fmaxf(y[r], 1e-6f); }
        p += e;
        if (t < 3) *(u32x2*)(sm + A_PK + t * 4352 + so) = pk4(p);
        else *(u32x2*)(sm + A_Q + w * 1536 + (16 * (t - 4) + m) * 32 + g * 8) = pk4(p);
      }
      if (t >= 2 && t <= 4) {
        u32x2 u = pk4(v[t] * rs);
        if (t == 4 && m == 8) u = (u32x2){0x3f803f80u, 0x3f803f80u};
        *(u32x2*)(sm + A_V + (t - 2) * 4352 + so) = u;
      }
    }
  }
  BAR();
  const rsrc_t grs = mkrs(ws + O_G + (LAST ? GL : 0) + hp * 16 * GP, 16 * GP);
  {
    f32x4 c = Z4;
#pragma unroll
    for (int k = 0; k < 4; ++k)
      c = mfma(ldf(sm + A_PK + pa * 4352 + m * 272 + k * 64 + g * 16), ldf(sm + A_V + pb * 4352 + m * 272 + k * 64 + g * 16), c);
    u32x4 u = ((__builtin_bit_cast(u32x4, c) + 0x26u) & 0xffffff00u) | 0x5au;
    if (valid) __builtin_amdgcn_raw_buffer_store_b128(u, grs, slot + rb * GP, 0, 16);
  }
  FENCE();
  u32x4 r[16];
  u32x2 x3v[3];
  const int sf = __shfl(slot, __ffsll((unsigned long long)__ballot(valid)) - 1), sl = valid ? slot : sf;
  if (!LAST) {
    WAITV(0);
#pragma unroll
    for (int p = 0; p < 16; ++p) r[p] = __builtin_amdgcn_raw_buffer_load_b128(grs, sl, p * GP, 16);
  }
  if (LAST) {
    WAITV(1); BAR();
    f32x4 y[2][3] = {};
#pragma unroll
    for (int i = 0; i < 9; ++i)
#pragma unroll
      for (int t = 0; t < 3; ++t) {
        bf16x8 qf = ldf(sm + A_QT + (t * 18 + 2 * i + kh) * 1024 + l16);
        y[0][t] = mfma(qf, xf[0][i], y[0][t]); y[1][t] = mfma(qf, xf[1][i], y[1][t]);
      }
    SB();
#pragma unroll
    for (int p = 0; p < 16; ++p) r[p] = __builtin_amdgcn_raw_buffer_load_b128(grs, sl, p * GP, 16);
    SB();
    char* ex = sm + A_PK;
#pragma unroll
    for (int t = 0; t < 3; ++t) *(f32x4*)(ex + (w * 3 + t) * 1024 + l16) = y[1][t];
    BAR();
#pragma unroll
    for (int t = 0; t < 3; ++t) {
      x3v[t] = pk4(y[0][t] + *(const f32x4*)(ex + ((w ^ 4) * 3 + t) * 1024 + l16));
      PIN(x3v[t]);
    }
  }
  FENCE();
  {
    for (int spin = 0; spin < 1000000; ++spin) {
      bool bad = false;
#pragma unroll
      for (int p = 0; p < 16; ++p)
        if (!__all(((r[p][0] & r[p][1]) & (r[p][2] & r[p][3]) & 0xffu) == 0x5au)) {
          FENCE();
          r[p] = __builtin_amdgcn_raw_buffer_load_b128(grs, sl, p * GP, 16);
          bad = true;
        }
      if (!bad) break;
    }
    if (valid) {
      f32x4 sum = Z4;
#pragma unroll
      for (int p = 0; p < 16; ++p) sum += __builtin_bit_cast(f32x4, r[p]);
      u32x2 u = pk4(sum);
      if (isz ? dq <= 4 : !eh) *(u32x2*)(sm + A_KV + (isz ? 18 : ec) * 64 + 16 + 8 * dq) = (u32x2){u[0], dq == 4 ? 0u : u[1]};
      if (isz ? dq >= 4 : eh) *(u32x2*)(sm + A_KV + (isz ? 50 : ec + 14) * 64 - 16 + 8 * dq) = (u32x2){dq == 4 ? 0u : u[0], u[1]};
    }
  }
  if (LAST) {
#pragma unroll
    for (int t = 0; t < 3; ++t)
      if (t < 2 || g == 0) { const int n = 36 * hp + 16 * t + 4 * g; Sp2{ws + O_X3 + ((n >> 3) * S + s0 + 16 * kh + m) * 16 + (n & 4) * 2} = x3v[t]; }
  }
  BAR();
  {
    unsigned tail[2];
    char* ob = ws + (LAST ? O_OB2 : O_OB) + (s0 + 16 * kh + m) * 16;
#pragma unroll
    for (int h = 0; h < 2; ++h) {
      bf16x8 k0 = ldf(sm + A_KV + (32 * h + m) * 64 + g * 16), k1 = ldf(sm + A_KV + (32 * h + 16 + m) * 64 + g * 16);
      bf16x8 pf = trf(sm + A_Q + w * 1536 + (16 * h + 8 * g + (m >> 2)) * 32 + (m & 3) * 8, 128);
      f32x4 n0 = mfma(k0, pf, Z4), n1 = mfma(k1, pf, Z4);
      float inv = __builtin_amdgcn_rcpf(__shfl(n1[2], m) + 1e-6f);
      int P = 9 * hp + 4 * h + g;
      Sp2{ob + (P >> 1) * (S * 16) + (P & 1) * 8} = pk4(n0 * inv);
      tail[h] = pk2(n1[0] * inv, n1[1] * inv);
    }
    int P = 9 * hp + 8;
    if (g == 0) Sp2{ob + (P >> 1) * (S * 16) + (P & 1) * 8} = (u32x2){tail[0], tail[1]};
  }
  FENCE();
}

__global__ void __launch_bounds__(512, 2) k_out(char* ws, char* outp, long layer) {
  const int MODE = 2 + (int)layer;
  const char* obp = ws + (MODE == 2 ? O_OB : O_OB2);
  extern __shared__ __attribute__((aligned(16))) char sm[];
  const int tid = threadIdx.x, w = __builtin_amdgcn_readfirstlane(tid >> 6), l = tid & 63, g = l >> 4, m = l & 15;
  const int b = blockIdx.x, rb = 8 * ((b & 7) >> 1) + ((b >> 3) & 7), ng = 3 * (b & 1) + (b >> 6);
  const int wr = w & 1, ch = (w >> 1) & 1, kh = w >> 2, s = 64 * rb + 32 * wr + 16 * kh + m;
  const rsrc_t wrs = mkrs(ws + (MODE == 2 ? O_WOQ : O_WOF) + ng * 110592, 110592);
  const int l16 = l * 16, xo0 = (g * S + s) * 16, xo1 = xo0 ^ 256;
  bf16x8 xf[2][9];
  auto issue1 = [&](int st, int p) {
    if (p < 3) {
      if (st == 4 && p == 2) return;
      int q = w + 8 * p; if (st == 4) q = q < 12 ? q : q - 8;
      int pc = st < 4 ? (q >> 2) * 18 + 4 * st + (q & 3) : (q >> 1) * 18 + 16 + (q & 1);
      bglds(wrs, sm + pc * 1024, l16, pc * 1024);
    } else {
      const int i = 2 * st + ((p - 3) >> 1);
      if (i > 8) return;
      const char* xb = obp + (2 * i + kh) * (4 * S * 16);
      if (p & 1) ALD16(xf[0][i], xo0, xb, 0); else ALD16(xf[1][i], xo1, xb, 0);
    }
    FENCE(); SB();
  };
  auto issue = [&](int st) {
#pragma unroll
    for (int p = 0; p < 7; ++p) issue1(st, p);
  };
  issue(0); issue(1);
  u32x2 rv[3];
  int ro[3];
#pragma unroll
  for (int t = 0; t < 3; ++t) {
    int n = 16 * (6 * ng + 3 * ch + t) + 4 * g;
    ro[t] = ((n >> 3) * S + s) * 16 + (n & 4) * 2;
  }
  f32x4 acc[6] = {};
  bf16x8 wf[2][6];
  const char* wa = sm + (54 * ch + kh) * 1024 + l16;
  WAITV(7); BAR();
#pragma unroll
  for (int t = 0; t < 6; ++t) wf[0][t] = ldf(wa + ((t % 3) * 18 + 2 * (t / 3)) * 1024);
  issue(2);
#pragma unroll
  for (int st = 0; st < 5; ++st) {
    if (st < 4) {
      if (st < 3) WAITV(7); else WAITV(3);
      BAR();
    }
#pragma unroll
    for (int k = 0; k < (st < 4 ? 2 : 1); ++k) {
      PIN(xf[0][2 * st + k]); PIN(xf[1][2 * st + k]);
#pragma unroll
      for (int t = 0; t < 3; ++t) {
        acc[t] = mfma(wf[st & 1][3 * k + t], xf[0][2 * st + k], acc[t]); acc[3 + t] = mfma(wf[st & 1][3 * k + t], xf[1][2 * st + k], acc[3 + t]);
        SB();
        if (st < 3 || (st == 3 && k == 0)) wf[(st + 1) & 1][3 * k + t] = ldf(wa + (t * 18 + 4 * st + 4 + 2 * k) * 1024);
        SB();
        if (st < 2) { issue1(st + 3, 3 * k + t); if (k == 1 && t == 2) issue1(st + 3, 6); }
        if (st == 1 && k == 1 && t == 2) {
          const char* rp = ws + (MODE == 2 ? O_XH1 : O_X3);
          ALD8(rv[0], ro[0], rp); ALD8(rv[1], ro[1], rp); ALD8(rv[2], ro[2], rp);
        }
      }
    }
  }
  BAR();
#pragma unroll
  for (int t = 0; t < 3; ++t) *(f32x4*)(sm + (w * 3 + t) * 1024 + l16) = acc[3 + t];
  WAITV(0); BAR();
  char* op = MODE == 2 ? ws + O_XH2 : outp;
  float sq = 0.f;
#pragma unroll
  for (int t = 0; t < 3; ++t) {
    PIN(rv[t]);
    f32x4 v = acc[t] + *(const f32x4*)(sm + ((w ^ 4) * 3 + t) * 1024 + l16);
    v[0] += lo16(rv[t][0]); v[1] += hi16(rv[t][0]); v[2] += lo16(rv[t][1]); v[3] += hi16(rv[t][1]);
    if (MODE == 2) St2{op + ro[t]} = pk4(v); else Sp2{op + s * 1152 + (16 * (6 * ng + 3 * ch + t) + 4 * g) * 2} = pk4(v);
    if (MODE == 2) sq += v[0] * v[0] + v[1] * v[1] + v[2] * v[2] + ((t == 2 && ng == 5 && ch && g == 3) ? 0.f : v[3] * v[3]);
  }
  if (MODE == 2) {
    sq += __shfl_xor(sq, 16); sq += __shfl_xor(sq, 32);
    float* red = (float*)(sm + 32768);
    if (ch && g == 0) red[(2 * wr + kh) * 16 + m] = sq;
    BAR();
    if (!ch && g == 0) ((float*)(ws + O_SS2))[ng * S + s] = sq + red[(2 * wr + kh) * 16 + m];
  }
  FENCE();
}

__global__ void __launch_bounds__(768, 6) k_pre(const char* x0, const char* wo, const char* wr, const char* wi, const char* wqkv, char* ws) {
  extern __shared__ __attribute__((aligned(16))) char sm[];
  const int b = blockIdx.x, tid = threadIdx.x;
  if (b < 192) {
    int x = b & 7, i = b >> 3;
    gemm_q<0>(sm, 4 * x + (i & 3), i >> 2, x0, ws + O_XH0, (float*)(ws + O_SS0));
  } else if (b < 246) {
    int u = b - 192;
    gemm_q<1>(sm, u % 9, u / 9, wo, ws + O_WOQ, nullptr);
  } else {
    int u = b - 246, tg = u >> 3, kb = 8 * (u & 7);
    u32x4 v[4], code;
    u32x2 wpf;
    {
      const char* pc; int len;
      asm volatile("s_getpc_b64 %0\n.Lpre_pc:\n\ts_mov_b32 %1, .Lpre_pc-k_fft1" : "=s"(pc), "=s"(len));
      int off = tg * 12288 + tid * 16;
      off = off + 16 <= len ? off : 0;
      const char* base = len > 0 ? pc - len : pc;
      ALD16(code, off, base, 0);
      const int wq = (u * 243 + (tid < 243 ? tid : 0)) * 128;
      ALD8(wpf, wq, wqkv);
    }
#pragma unroll
    for (int i = 0; i < 4; ++i) {
      int e = i * 768 + tid; e = e < 2312 ? e : 2311;
      int run = e / 289, o = e - 289 * run;
      v[i] = *(const u32x4*)((run < 4 ? wr : wi) + (kb + 64 * (4 * tg + (run & 3))) * 578 + o * 16);
    }
    {
      const int id = u * 768 + tid;
      St4{ws + O_G + id * 16} = (u32x4){0u, 0u, 0u, 0u};
      if (id < 4608) St4{ws + O_WF + ((7 * (id / 288) + 6) * 18 + (id % 288 >> 4)) * 1024 + (12 + (id & 3) + 16 * ((id >> 2) & 3)) * 16} = (u32x4){0u, 0u, 0u, 0u};
    }
#pragma unroll
    for (int i = 0; i < 4; ++i) { int e = i * 768 + tid; e = e < 2312 ? e : 2311; *(u32x4*)(sm + e * 16) = v[i]; }
    __syncthreads();
#pragma unroll
    for (int i = 0; i < 3; ++i) {
      int e = i * 768 + tid, mm = e & 7, c = e >> 3;
      const char* p = sm + mm * 578 + 2 * (c + 1);
      u32x4 o;
#pragma unroll
      for (int r = 0; r < 4; ++r) o[r] = (unsigned)*(const u16*)(p + r * 4624) | ((unsigned)*(const u16*)(p + (4 + r) * 4624) << 16);
      St4{ws + O_WT + ((c * 4 + (kb >> 4)) * 2 + (tg >> 2)) * 1024 + (16 * (tg & 3) + (kb & 8) + mm) * 16} = o;
    }
    WAITV(0); PIN(code); PIN(wpf);
    FENCE();
  }
}

extern "C" void kernel_launch(void* const* d_in, const int* in_sizes, int n_in, void* d_out, int out_size, void* d_ws,
                              size_t ws_size, hipStream_t stream) {
  const char *x0 = (const char*)d_in[0], *wr = (const char*)d_in[1], *wi = (const char*)d_in[2];
  const char *wqkv = (const char*)d_in[4], *wo = (const char*)d_in[5];
  char* ws = (char*)d_ws;
  (void)hipFuncSetAttribute((const void*)k_pre, hipFuncAttributeMaxDynamicSharedMemorySize, 73728);
  (void)hipFuncSetAttribute((const void*)k_fft1, hipFuncAttributeMaxDynamicSharedMemorySize, 73728);
  (void)hipFuncSetAttribute((const void*)k_attn, hipFuncAttributeMaxDynamicSharedMemorySize, 163328);
  (void)hipFuncSetAttribute((const void*)k_out, hipFuncAttributeMaxDynamicSharedMemorySize, 110592);
  k_pre<<<310, 768, 73728, stream>>>(x0, wo, wr, wi, wqkv, ws);
  k_fft1<<<252, 512, 73728, stream>>>(wqkv, ws, 0L);
  k_attn<<<256, 512, 163328, stream>>>(ws, 0L);
  k_out<<<192, 512, 110592, stream>>>(ws, (char*)d_out, 0L);
  k_fft1<<<252, 512, 40960, stream>>>(wo, ws, 1L);
  k_attn<<<256, 512, 163328, stream>>>(ws, 1L);
  k_out<<<192, 512, 110592, stream>>>(ws, (char*)d_out, 1L);
}
```
